```python
import torch

def _softmax(x, axis=-1):
    return torch.softmax(x, dim=axis)
def _gelu(x):
    return torch.nn.functional.gelu(x, approximate="tanh")
def _silu(x):
    return torch.nn.functional.silu(x)
def _sigmoid(x):
    return torch.sigmoid(x)
def _layernorm(x, axis=-1, eps=1e-5):
    m = x.mean(dim=axis, keepdim=True)
    v = x.var(dim=axis, keepdim=True, unbiased=False)
    return (x - m) / torch.sqrt(v + eps)
def _scatter_add(tgt, idx, src, axis=0):
    return tgt.clone().index_add_(axis, idx, src)
def _segment_sum(src, seg, n):
    out = torch.zeros((n,) + src.shape[1:], dtype=src.dtype)
    return out.index_add_(0, seg, src)
def _pad(x, pw):
    flat = tuple(v for pair in reversed(pw) for v in pair)
    return torch.nn.functional.pad(x, flat)

kernel_name = 'gated_mix__sliding_block_cached__glu_gated_geglu__decode'

def setup_inputs():

    _w = (lambda a: (lambda t: t.to(torch.bfloat16) if t.is_floating_point() else t)(torch.as_tensor(a)))
    def _A_setup():
        B, Sq, D = 4, 1, 640
        Hq, Hkv, Sk, Dh = 32, 32, 4096, 128
        assert Hq % Hkv == 0, (Hq, Hkv)
        return {
            "x": _w((lambda *s: torch.randn(*s, dtype=torch.bfloat16) * 0.05)(B, Sq, D)),
            "k_cache": _w((lambda *s: torch.randn(*s, dtype=torch.bfloat16) * 6.4)(B, Hkv, Sk, Dh)),
            "v_cache": _w((lambda *s: torch.randn(*s, dtype=torch.bfloat16) * 0.05)(B, Hkv, Sk, Dh)),
            "wq": _w((lambda *s: torch.randn(*s, dtype=torch.bfloat16) * 6.4)(D, Hq * Dh) * (D ** -0.5)),
            "wo": _w((lambda *s: torch.randn(*s, dtype=torch.bfloat16) * 0.05)(Hq * Dh, D) * ((Hq * Dh) ** -0.5)),
        }
    _A_in = _A_setup()

    def _B_setup():
        B, S, Dm, Df = 4, 1, 640, 1728
        return {
            "x": _w((lambda *s: torch.randn(*s, dtype=torch.bfloat16) * 0.05)(B, S, Dm)),
            "w_gate": _w((lambda *s: torch.randn(*s, dtype=torch.bfloat16) * 0.8)(Dm, Df) * (Dm ** -0.5)),
            "w_up": _w((lambda *s: torch.randn(*s, dtype=torch.bfloat16) * 0.8)(Dm, Df) * (Dm ** -0.5)),
            "w_down": _w((lambda *s: torch.randn(*s, dtype=torch.bfloat16) * 0.8)(Df, Dm) * (Df ** -0.5)),
        }
    _B_in = _B_setup()

    return {
        "A_x": _A_in["x"],
        "A_k_cache": _A_in["k_cache"],
        "A_v_cache": _A_in["v_cache"],
        "A_wq": _A_in["wq"],
        "A_wo": _A_in["wo"],
        "B_x": _B_in["x"],
        "B_w_gate": _B_in["w_gate"],
        "B_w_up": _B_in["w_up"],
        "B_w_down": _B_in["w_down"],
        "gate_w": _w((lambda *s: torch.randn(*s, dtype=torch.bfloat16) * 0.05)(640) * 16.0),
    }

def reference(A_x, A_k_cache, A_v_cache, A_wq, A_wo, B_x, B_w_gate, B_w_up, B_w_down, gate_w):
    def _A_ref(x, k_cache, v_cache, wq, wo):
        B, Sq, D = x.shape
        Hq, Dh = 32, 128
        Hkv, Sk = k_cache.shape[1], k_cache.shape[2]
        W = min(4096, max(256, Sk // 4))
        rep = Hq // Hkv
        q = (lambda x, *d: x.permute(*d))((x @ wq).reshape(B, Sq, Hq, Dh), 0, 2, 1, 3)
        k = (lambda x, n, axis: torch.repeat_interleave(x, n, dim=axis))(k_cache[:, :, Sk - W:, :], rep, axis=1)
        v = (lambda x, n, axis: torch.repeat_interleave(x, n, dim=axis))(v_cache[:, :, Sk - W:, :], rep, axis=1)
        scores = torch.einsum("bhqd,bhkd->bhqk", q, k) * (Dh ** -0.5)
        p = _softmax(scores, axis=-1)
        o = torch.einsum("bhqk,bhkd->bhqd", p, v)
        return (lambda x, *d: x.permute(*d))(o, 0, 2, 1, 3).reshape(B, Sq, Hq * Dh) @ wo

    def _B_ref(x, w_gate, w_up, w_down):
        g = torch.einsum("bsd,df->bsf", x, w_gate)
        u = torch.einsum("bsd,df->bsf", x, w_up)
        h = _gelu(g) * u
        return torch.einsum("bsf,fd->bsd", h, w_down)

    x = A_x
    g = _sigmoid((x * gate_w).sum(-1, keepdims=True))
    a = _A_ref(x, A_k_cache, A_v_cache, A_wq, A_wo)
    b = _B_ref(x, B_w_gate, B_w_up, B_w_down)
    return g * a + (1 - g) * b
```

```cpp
#include <hip/hip_runtime.h>
#include <hip/hip_bf16.h>
#include <stdint.h>

typedef unsigned u32;
using u32x4 = __attribute__((ext_vector_type(4))) unsigned;
using u32x2 = __attribute__((ext_vector_type(2))) unsigned;
using f32x4 = __attribute__((ext_vector_type(4))) float;
using f32x2 = __attribute__((ext_vector_type(2))) float;
using bf16x2 = __attribute__((ext_vector_type(2))) __bf16;

constexpr int DM = 640, NH = 32, SK = 4096, WIN = 1024, DF = 1728;
constexpr int NTHR = 576;
constexpr int AUX_SC1 = 16;
constexpr u32 SPIN_CAP = 1u << 18;

constexpr int WS_E = 0;
constexpr int WS_QP = 4096;
constexpr int WS_GP = WS_QP + 32 * 4 * 8 * 128 * 8;
constexpr int WS_SX = WS_GP + 2 * 4 * 8 * DF * 8;
constexpr int WS_YP = WS_SX + 256 * 8 * 64 * 8;
constexpr int WS_END = WS_YP + 4 * 64 * DM * 8;

constexpr int L_XS = 0;
constexpr int L_R1 = L_XS + 8 * 160;
constexpr int L_QS = L_R1 + 2048 + 1024;
constexpr int L_HS = L_QS + 64;
constexpr int L_MISC = L_HS + 32;
constexpr int L_SW = L_MISC + 4;
constexpr int L_REC = L_SW + 512;
constexpr int L_YR = L_REC + 8 * 68;
constexpr int L_END = L_YR + 8 * DM;

struct Params {
  const char* x; const char* k; const char* v; const char* wq; const char* wo;
  const char* wg; const char* wu; const char* wd; const char* gw; char* out; char* ws;
};

#define SB() __builtin_amdgcn_sched_barrier(0)
#define CLOB() asm volatile("" ::: "memory")
#define WG_BARRIER() do { asm volatile("s_waitcnt lgkmcnt(0)" ::: "memory"); __builtin_amdgcn_s_barrier(); CLOB(); } while (0)

__device__ __forceinline__ u32x4 ldnt(const char* p) { return __builtin_nontemporal_load(reinterpret_cast<const u32x4*>(p)); }
__device__ __forceinline__ float asf(u32 x) { return __builtin_bit_cast(float, x); }
__device__ __forceinline__ u32 asu(float x) { return __builtin_bit_cast(u32, x); }
__device__ __forceinline__ float lo16(u32 x) { return asf(x << 16); }
__device__ __forceinline__ float hi16(u32 x) { return asf(x & 0xffff0000u); }
__device__ __forceinline__ float dot2(u32 a, u32 b, float acc) {
  return __builtin_amdgcn_fdot2_f32_bf16(__builtin_bit_cast(bf16x2, a), __builtin_bit_cast(bf16x2, b), acc, false);
}
__device__ __forceinline__ u32 pkbf(float lo, float hi) {
  bf16x2 r; r[0] = (__bf16)lo; r[1] = (__bf16)hi; return __builtin_bit_cast(u32, r);
}
__device__ __forceinline__ float rbf(float x) { return lo16(pkbf(x, 0.f)); }
template <int CTRL> __device__ __forceinline__ float dpp(float x) {
  return asf(__builtin_amdgcn_mov_dpp(__builtin_bit_cast(int, x), CTRL, 0xf, 0xf, true));
}
constexpr int XOR1 = 0xB1, XOR2 = 0x4E, XOR7 = 0x141, XOR8 = 0x128;
__device__ __forceinline__ float red32(float a, float b) {
  auto s = __builtin_amdgcn_permlane32_swap(asu(a), asu(b), false, false); return asf(s[0]) + asf(s[1]);
}
__device__ __forceinline__ float red16(float a, float b) {
  auto s = __builtin_amdgcn_permlane16_swap(asu(a), asu(b), false, false); return asf(s[0]) + asf(s[1]);
}
__device__ __forceinline__ float max32(float a) {
  auto s = __builtin_amdgcn_permlane32_swap(asu(a), asu(a), false, false); return fmaxf(asf(s[0]), asf(s[1]));
}
__device__ __forceinline__ float max16(float a) {
  auto s = __builtin_amdgcn_permlane16_swap(asu(a), asu(a), false, false); return fmaxf(asf(s[0]), asf(s[1]));
}

__device__ __forceinline__ void pv_step(float (&acc)[8], float p, const u32x4& v) {
#pragma unroll
  for (int k = 0; k < 4; ++k) { acc[2 * k] = fmaf(p, lo16(v[k]), acc[2 * k]); acc[2 * k + 1] = fmaf(p, hi16(v[k]), acc[2 * k + 1]); }
}
__device__ __forceinline__ void gemv_step(float (&acc)[8], u32 mlo, const u32x4& w) {
  const u32 mhi = mlo << 16;
#pragma unroll
  for (int k = 0; k < 4; ++k) { acc[2 * k] = dot2(w[k], mlo, acc[2 * k]); acc[2 * k + 1] = dot2(w[k], mhi, acc[2 * k + 1]); }
}

__global__ void __launch_bounds__(NTHR, 3) fused_decode(Params p) {
  __shared__ __attribute__((aligned(16))) float lds[L_END];
  const int bid = blockIdx.x, tid = threadIdx.x, lane = tid & 63;
  const int wave = __builtin_amdgcn_readfirstlane(tid >> 6);
  const int xcd = bid & 7, slot = bid >> 3;
  const int h = xcd * 4 + (slot >> 3), t = slot & 7, b = t >> 1, j = t & 1;
  const int wg = h * 8 + t, pr = h * 2 + j;
  const __amdgpu_buffer_rsrc_t rs = __builtin_amdgcn_make_buffer_rsrc((void*)p.ws, 0, WS_END, 0x00020000);
  u32* ldsu = reinterpret_cast<u32*>(lds);

  if (wave == 8) {
    __builtin_amdgcn_s_setprio(3);
    const u32 tag = __builtin_amdgcn_readfirstlane(__builtin_amdgcn_raw_buffer_load_b32(rs, WS_E, 0, AUX_SC1)) + 1u;
    {
      float s = 0.f;
#pragma unroll
      for (int i = 0; i < 5; ++i) {
        const u32 xv = *reinterpret_cast<const u32*>(p.x + b * (DM * 2) + (i * 64 + lane) * 4);
        const u32 gv = *reinterpret_cast<const u32*>(p.gw + (i * 64 + lane) * 4);
        s = dot2(xv, gv, s);
      }
      s += dpp<XOR1>(s); s += dpp<XOR2>(s); s += dpp<XOR7>(s); s += dpp<XOR8>(s);
      s = red16(s, s); s = red32(s, s);
      if (lane == 0) { ldsu[L_MISC] = tag; lds[L_MISC + 1] = 1.f / (1.f + __expf(-s)); }
    }
    WG_BARRIER();
    {
      float q0, q1;
      for (u32 spins = 0;; ++spins) {
        CLOB();
        u32x4 g[8];
#pragma unroll
        for (int u = 0; u < 8; ++u) g[u] = __builtin_amdgcn_raw_buffer_load_b128(rs, WS_QP + ((h * 4 + b) * 8 + u) * 1024 + lane * 16, 0, AUX_SC1);
        bool ok = true; q0 = 0.f; q1 = 0.f;
#pragma unroll
        for (int u = 0; u < 8; ++u) { ok &= (g[u][1] == tag) & (g[u][3] == tag); q0 += asf(g[u][0]); q1 += asf(g[u][2]); }
        if (__all(ok) || spins > SPIN_CAP) break;
        __builtin_amdgcn_s_sleep(4);
      }
      ldsu[L_QS + lane] = pkbf(q0, q1);
    }
    WG_BARRIER();
    {
      const int i = lane & 31, m = lane >> 5, f = 27 * pr + (i < 27 ? i : 26);
      float a;
      for (u32 spins = 0;; ++spins) {
        CLOB();
        u32x2 g[8];
#pragma unroll
        for (int u = 0; u < 8; ++u) g[u] = __builtin_amdgcn_raw_buffer_load_b64(rs, WS_GP + (((m * 4 + b) * 8 + u) * DF + f) * 8, 0, AUX_SC1);
        bool ok = true; a = 0.f;
#pragma unroll
        for (int u = 0; u < 8; ++u) { ok &= (g[u][1] == tag); a += asf(g[u][0]); }
        if (__all(ok) || spins > SPIN_CAP) break;
        __builtin_amdgcn_s_sleep(4);
      }
      a = rbf(a);
      auto s = __builtin_amdgcn_permlane32_swap(asu(a), asu(a), false, false);
      const float gg = asf(s[0]), uu = asf(s[1]);
      const float ge = rbf(0.5f * gg * (1.f + tanhf(0.7978845608028654f * (gg + 0.044715f * gg * gg * gg))));
      if (lane < 32) ldsu[L_HS + lane] = (i < 27) ? (pkbf(ge * uu, 0.f) & 0xffffu) : 0u;
    }
    if (wg == 255) {
      for (u32 spins = 0;; ++spins) {
        CLOB();
        bool ok = true;
#pragma unroll
        for (int u = 0; u < 4; ++u) { const int w2 = u * 64 + lane;
          const u32x2 g = __builtin_amdgcn_raw_buffer_load_b64(rs, WS_QP + ((w2 >> 3) * 32 + (w2 & 7)) * 1024, 0, AUX_SC1);
          ok &= (g[1] == tag); }
        if (__all(ok) || spins > SPIN_CAP) break;
        __builtin_amdgcn_s_sleep(4);
      }
      if (lane == 0) __builtin_amdgcn_raw_buffer_store_b32(tag, rs, WS_E, 0, AUX_SC1);
    }
    WG_BARRIER();
    WG_BARRIER();
    if (pr < 40) {
      float y0, y1;
      for (u32 spins = 0;; ++spins) {
        CLOB();
        u32x4 g[8];
#pragma unroll
        for (int u = 0; u < 8; ++u)
          g[u] = __builtin_amdgcn_raw_buffer_load_b128(rs, WS_YP + ((b * 64 + u * 8 + (lane >> 3)) * DM + 16 * pr + 2 * (lane & 7)) * 8, 0, AUX_SC1);
        bool ok = true; y0 = 0.f; y1 = 0.f;
#pragma unroll
        for (int u = 0; u < 8; ++u) { ok &= (g[u][1] == tag) & (g[u][3] == tag); y0 += asf(g[u][0]); y1 += asf(g[u][2]); }
        if (__all(ok) || spins > SPIN_CAP) break;
        __builtin_amdgcn_s_sleep(1);
      }
      y0 += dpp<XOR8>(y0); y1 += dpp<XOR8>(y1);
      y0 = red16(y0, y0); y1 = red16(y1, y1);
      y0 = red32(y0, y0); y1 = red32(y1, y1);
      if (lane < 8) *reinterpret_cast<u32*>(p.out + (b * DM + 16 * pr + 2 * lane) * 2) = pkbf(y0, y1);
    }
    return;
  }

  const bool isq = wave < 4;
  const bool s1on = isq || h < 27;
  const int row0 = 80 * t + (isq ? 20 * wave : 40 * (wave & 1));
  u32x4 S1[5], K[16], V[16], WD[5], WO[10];
  u32x2 xr = {0u, 0u};
  const int xb = lane / 10, xc = lane - 10 * xb;
  const bool xon = lane < 40 && xc < (isq ? 5 : 10);
  if (xon) xr = *reinterpret_cast<const u32x2*>(p.x + xb * (DM * 2) + (row0 + 4 * xc) * 2);
  {
    const char* sp; int sstep;
    if (isq) { sp = p.wq + (size_t)(row0 + (lane >> 4)) * 8192 + h * 256 + (lane & 15) * 16; sstep = 4 * 8192; }
    else { sp = (wave < 6 ? p.wg : p.wu) + (size_t)(row0 + (lane >> 3)) * (DF * 2) + (s1on ? h : 0) * 128 + (lane & 7) * 16; sstep = 8 * DF * 2; }
    if (s1on) {
#pragma unroll
      for (int i = 0; i < 5; ++i) S1[i] = ldnt(sp + i * sstep);
    } else {
#pragma unroll
      for (int i = 0; i < 5; ++i) S1[i] = u32x4{0u, 0u, 0u, 0u};
    }
  }
  const size_t kvbase = ((size_t)(b * NH + h) * SK + (SK - WIN)) * 256;
  const char* kp = p.k + kvbase + (size_t)(j * 512 + wave * 64) * 256 + lane * 16;
  const char* vpa = p.v + kvbase + (size_t)(j * 512 + wave * 64 + (lane >> 3)) * 256 + j * 128 + (lane & 7) * 16;
  const char* vpb = p.v + kvbase + (size_t)((j ^ 1) * 512 + wave * 64 + (lane >> 3)) * 256 + j * 128 + (lane & 7) * 16;
  SB();
#pragma unroll
  for (int i = 0; i < 8; ++i) K[i] = ldnt(kp + i * 1024);
  SB();
  {
    float* xs = lds + L_XS + wave * 160;
    if (xon) { xs[(4 * xc + 0) * 4 + xb] = lo16(xr[0]); xs[(4 * xc + 1) * 4 + xb] = hi16(xr[0]); xs[(4 * xc + 2) * 4 + xb] = lo16(xr[1]); xs[(4 * xc + 3) * 4 + xb] = hi16(xr[1]); }
    asm volatile("s_waitcnt lgkmcnt(0)" ::: "memory");
    float a[4][8];
#pragma unroll
    for (int bb = 0; bb < 4; ++bb)
#pragma unroll
      for (int k = 0; k < 8; ++k) a[bb][k] = 0.f;
    const int rl = isq ? (lane >> 4) : (lane >> 3), rstep = isq ? 4 : 8;
#pragma unroll
    for (int i = 0; i < 5; ++i) {
      const f32x4 xv = *reinterpret_cast<const f32x4*>(xs + (rl + i * rstep) * 4);
#pragma unroll
      for (int bb = 0; bb < 4; ++bb) pv_step(a[bb], xv[bb], S1[i]);
    }
    float r[8];
#pragma unroll
    for (int k = 0; k < 8; ++k) r[k] = red16(red32(a[0][k], a[2][k]), red32(a[1][k], a[3][k]));
    if (isq) {
      float* d = lds + L_R1 + (wave * 4 + (lane >> 4)) * 128 + (lane & 15) * 8;
      *reinterpret_cast<f32x4*>(d) = f32x4{r[0], r[1], r[2], r[3]}; *reinterpret_cast<f32x4*>(d + 4) = f32x4{r[4], r[5], r[6], r[7]};
    } else {
#pragma unroll
      for (int k = 0; k < 8; ++k) r[k] += dpp<XOR8>(r[k]);
      float* d = lds + L_R1 + 2048 + ((wave - 4) * 4 + (lane >> 4)) * 64 + (lane & 7) * 8 + ((lane & 8) >> 1);
      *reinterpret_cast<f32x4*>(d) = (lane & 8) ? f32x4{r[4], r[5], r[6], r[7]} : f32x4{r[0], r[1], r[2], r[3]};
    }
  }
  WG_BARRIER();
  const u32 tag = __builtin_amdgcn_readfirstlane(ldsu[L_MISC]);
  {
    f32x2 s; int off;
    if (isq) {
      const float* d = lds + L_R1 + wave * 128 + lane * 2;
      s = *reinterpret_cast<const f32x2*>(d) + *reinterpret_cast<const f32x2*>(d + 512) + *reinterpret_cast<const f32x2*>(d + 1024) + *reinterpret_cast<const f32x2*>(d + 1536);
      off = WS_QP + ((h * 4 + wave) * 8 + t) * 1024 + lane * 16;
    } else {
      const int m = (wave - 4) >> 1, bb = ((wave & 1) << 1) | (lane >> 5), cp = lane & 31;
      const float* d = lds + L_R1 + 2048 + (m * 8 + bb) * 64 + cp * 2;
      s = *reinterpret_cast<const f32x2*>(d) + *reinterpret_cast<const f32x2*>(d + 256);
      off = WS_GP + (((m * 4 + bb) * 8 + t) * DF + h * 64 + cp * 2) * 8;
    }
    if (s1on) __builtin_amdgcn_raw_buffer_store_b128(u32x4{asu(s[0]), tag, asu(s[1]), tag}, rs, off, 0, AUX_SC1);
  }
  SB();
#pragma unroll
  for (int i = 8; i < 16; ++i) K[i] = ldnt(kp + i * 1024);
#pragma unroll
  for (int i = 0; i < 4; ++i) V[i] = ldnt(vpa + i * 2048);
  SB();
  WG_BARRIER();
  float sa[8];
  {
    const u32x4 q = *reinterpret_cast<const u32x4*>(ldsu + L_QS + (lane & 15) * 4);
    float x[16];
#pragma unroll
    for (int i = 0; i < 16; ++i) { float d = 0.f;
#pragma unroll
      for (int k = 0; k < 4; ++k) d = dot2(K[i][k], q[k], d);
      x[i] = d; }
#define TR_STEP(HALF, CTRL) _Pragma("unroll") for (int u = 0; u < (HALF); ++u) { \
      const float t1_ = x[u] + dpp<CTRL>(x[u]), t2_ = x[u + (HALF)] + dpp<CTRL>(x[u + (HALF)]); x[u] = (lane & (HALF)) ? t2_ : t1_; }
    TR_STEP(8, XOR8) TR_STEP(4, XOR7) TR_STEP(2, XOR2) TR_STEP(1, XOR1)
#undef TR_STEP
    const float s = x[0] * (0.08838834764831845f * 1.4426950408889634f);
    const int key = 4 * (lane & 15) + (lane >> 4), pos = (key & 7) * 8 + (key >> 3);
    __builtin_amdgcn_raw_buffer_store_b64(u32x2{asu(s), tag}, rs, WS_SX + ((wg * 8 + wave) * 64 + pos) * 8, 0, AUX_SC1);
    float* sw = lds + L_SW + wave * 64;
    sw[pos] = s;
    asm volatile("s_waitcnt lgkmcnt(0)" ::: "memory");
    const f32x4 s0 = *reinterpret_cast<const f32x4*>(sw + (lane >> 3) * 8), s1 = *reinterpret_cast<const f32x4*>(sw + (lane >> 3) * 8 + 4);
#pragma unroll
    for (int k = 0; k < 4; ++k) { sa[k] = s0[k]; sa[4 + k] = s1[k]; }
  }
  float acc[8], m, l;
  {
    m = fmaxf(fmaxf(fmaxf(sa[0], sa[1]), fmaxf(sa[2], sa[3])), fmaxf(fmaxf(sa[4], sa[5]), fmaxf(sa[6], sa[7])));
    m = fmaxf(m, dpp<XOR8>(m)); m = max16(m); m = max32(m);
    l = 0.f;
#pragma unroll
    for (int k = 0; k < 8; ++k) { sa[k] = __builtin_amdgcn_exp2f(sa[k] - m); l += sa[k]; acc[k] = 0.f; }
  }
  SB();
#pragma unroll
  for (int i = 0; i < 8; ++i) {
    V[i + 4] = (i + 4 < 8) ? ldnt(vpa + (i + 4) * 2048) : ldnt(vpb + (i - 4) * 2048);
    SB();
    pv_step(acc, sa[i], V[i]);
    SB();
  }
  const int sxoff = WS_SX + (((wg ^ 1) * 8 + wave) * 64 + (lane >> 3) * 8) * 8;
  u32x4 g[4];
#pragma unroll
  for (int u = 0; u < 4; ++u) g[u] = __builtin_amdgcn_raw_buffer_load_b128(rs, sxoff + u * 16, 0, AUX_SC1);
#pragma unroll
  for (int i = 12; i < 16; ++i) V[i] = ldnt(vpb + (i - 8) * 2048);
  SB();
  for (u32 spins = 0;; ++spins) {
    bool ok = true;
#pragma unroll
    for (int u = 0; u < 4; ++u) ok &= (g[u][1] == tag) & (g[u][3] == tag);
    if (__all(ok) || spins > SPIN_CAP) break;
    __builtin_amdgcn_s_sleep(1);
    CLOB();
#pragma unroll
    for (int u = 0; u < 4; ++u) g[u] = __builtin_amdgcn_raw_buffer_load_b128(rs, sxoff + u * 16, 0, AUX_SC1);
  }
  {
#pragma unroll
    for (int u = 0; u < 4; ++u) { sa[2 * u] = asf(g[u][0]); sa[2 * u + 1] = asf(g[u][2]); }
    float m2 = fmaxf(fmaxf(fmaxf(sa[0], sa[1]), fmaxf(sa[2], sa[3])), fmaxf(fmaxf(sa[4], sa[5]), fmaxf(sa[6], sa[7])));
    m2 = fmaxf(m2, dpp<XOR8>(m2)); m2 = max16(m2); m2 = max32(m2);
    m2 = fmaxf(m, m2);
    const float al = __builtin_amdgcn_exp2f(m - m2);
    m = m2; l *= al;
#pragma unroll
    for (int k = 0; k < 8; ++k) { sa[k] = __builtin_amdgcn_exp2f(sa[k] - m); l += sa[k]; acc[k] *= al; }
  }
  const char* wdp = p.wd + (size_t)(27 * pr) * (DM * 2);
  const char* wop = p.wo + (size_t)(64 * pr + 8 * wave) * (DM * 2);
  SB();
#pragma unroll
  for (int i = 0; i < 4; ++i) {
    const int r = min(4 * wave + i, 26);
    WD[i] = ldnt(wdp + r * (DM * 2) + lane * 16);
    SB();
    pv_step(acc, sa[i], V[8 + i]);
    SB();
  }
  WD[4] = ldnt(wdp + min(4 * wave + (lane >> 4), 26) * (DM * 2) + 1024 + (lane & 15) * 16);
  SB();
#pragma unroll
  for (int i = 4; i < 8; ++i) pv_step(acc, sa[i], V[8 + i]);
  {
    float u4[4], u2[2];
#pragma unroll
    for (int k = 0; k < 4; ++k) u4[k] = red32(acc[k], acc[k + 4]);
#pragma unroll
    for (int k = 0; k < 2; ++k) u2[k] = red16(u4[k], u4[k + 2]);
    const float t1 = u2[0] + dpp<XOR8>(u2[0]), t2 = u2[1] + dpp<XOR8>(u2[1]);
    float* rec = lds + L_REC + wave * 68;
    rec[4 + 8 * (lane & 7) + (lane >> 3)] = (lane & 8) ? t2 : t1;
    l += dpp<XOR8>(l); l = red16(l, l); l = red32(l, l);
    if (lane == 0) *reinterpret_cast<f32x2*>(rec) = f32x2{m, l};
  }
  SB();
#pragma unroll
  for (int i = 0; i < 8; ++i) WO[i] = ldnt(wop + i * (DM * 2) + lane * 16);
#pragma unroll
  for (int i = 0; i < 2; ++i) WO[8 + i] = ldnt(wop + (4 * i + (lane >> 4)) * (DM * 2) + 1024 + (lane & 15) * 16);
  SB();
  WG_BARRIER();
  float ym[8], yr[8], om[8], orr[8];
#pragma unroll
  for (int k = 0; k < 8; ++k) { ym[k] = 0.f; yr[k] = 0.f; om[k] = 0.f; orr[k] = 0.f; }
  u32 ob;
  {
    const float* rec = lds + L_REC + (lane & 7) * 68;
    const f32x2 ml = *reinterpret_cast<const f32x2*>(rec);
    const float a = rec[4 + 8 * wave + (lane >> 3)];
    float M = ml[0];
    M = fmaxf(M, dpp<XOR1>(M)); M = fmaxf(M, dpp<XOR2>(M)); M = fmaxf(M, dpp<XOR7>(M));
    const float f = __builtin_amdgcn_exp2f(ml[0] - M);
    float L = f * ml[1], o = f * a;
    L += dpp<XOR1>(L); o += dpp<XOR1>(o); L += dpp<XOR2>(L); o += dpp<XOR2>(o); L += dpp<XOR7>(L); o += dpp<XOR7>(o);
    ob = pkbf(o / L, 0.f) & 0xffffu;
  }
  const float gate = lds[L_MISC + 1];
  {
    const u32x4 hv = *reinterpret_cast<const u32x4*>(ldsu + L_HS + 4 * wave);
    const u32 hr = ldsu[L_HS + 4 * wave + (lane >> 4)];
#pragma unroll
    for (int i = 0; i < 4; ++i) gemv_step(ym, hv[i], WD[i]);
    gemv_step(yr, hr, WD[4]);
#pragma unroll
    for (int k = 0; k < 8; ++k) { ym[k] *= (1.f - gate); yr[k] *= (1.f - gate); }
  }
  {
    u32 os[8];
#pragma unroll
    for (int i = 0; i < 8; ++i) os[i] = __builtin_amdgcn_readlane(ob, 8 * i);
#pragma unroll
    for (int i = 0; i < 8; ++i) gemv_step(om, os[i], WO[i]);
    const int r = lane >> 4;
#pragma unroll
    for (int i = 0; i < 2; ++i) {
      const u32 mr = r == 0 ? os[4 * i] : r == 1 ? os[4 * i + 1] : r == 2 ? os[4 * i + 2] : os[4 * i + 3];
      gemv_step(orr, mr, WO[8 + i]);
    }
#pragma unroll
    for (int k = 0; k < 8; ++k) { ym[k] = fmaf(gate, om[k], ym[k]); yr[k] = fmaf(gate, orr[k], yr[k]); }
    float* y = lds + L_YR + wave * DM;
    *reinterpret_cast<f32x4*>(y + lane * 8) = f32x4{ym[0], ym[1], ym[2], ym[3]};
    *reinterpret_cast<f32x4*>(y + lane * 8 + 4) = f32x4{ym[4], ym[5], ym[6], ym[7]};
    float u4[4];
#pragma unroll
    for (int k = 0; k < 4; ++k) u4[k] = red32(yr[k], yr[k + 4]);
    *reinterpret_cast<f32x2*>(y + 512 + 8 * (lane & 15) + 2 * r) = f32x2{red16(u4[0], u4[2]), red16(u4[1], u4[3])};
  }
  WG_BARRIER();
  if (tid < 320) {
    const float* y = lds + L_YR + tid * 2;
    f32x2 s = *reinterpret_cast<const f32x2*>(y);
#pragma unroll
    for (int w = 1; w < 8; ++w) s += *reinterpret_cast<const f32x2*>(y + w * DM);
    __builtin_amdgcn_raw_buffer_store_b128(u32x4{asu(s[0]), tag, asu(s[1]), tag}, rs, WS_YP + ((b * 64 + pr) * DM + tid * 2) * 8, 0, AUX_SC1);
  }
}

extern "C" void kernel_launch(void* const* d_in, const int* in_sizes, int n_in, void* d_out, int out_size, void* d_ws, size_t ws_size,
                              hipStream_t stream) {
  Params p;
  p.x = (const char*)d_in[0]; p.k = (const char*)d_in[1]; p.v = (const char*)d_in[2]; p.wq = (const char*)d_in[3]; p.wo = (const char*)d_in[4];
  p.wg = (const char*)d_in[6]; p.wu = (const char*)d_in[7]; p.wd = (const char*)d_in[8]; p.gw = (const char*)d_in[9];
  p.out = (char*)d_out; p.ws = (char*)d_ws;
  hipLaunchKernelGGL(fused_decode, dim3(256), dim3(NTHR), 0, stream, p);
}
```

```cpp
#include <hip/hip_runtime.h>
#include <hip/hip_bf16.h>
#include <stdint.h>

typedef unsigned u32;
using u32x4 = __attribute__((ext_vector_type(4))) unsigned;
using u32x2 = __attribute__((ext_vector_type(2))) unsigned;
using f32x4 = __attribute__((ext_vector_type(4))) float;
using f32x2 = __attribute__((ext_vector_type(2))) float;
using bf16x2 = __attribute__((ext_vector_type(2))) __bf16;
using bf16x8 = __attribute__((ext_vector_type(8))) __bf16;

constexpr int DM = 640, NH = 32, SK = 4096, WIN = 1024, DF = 1728;
constexpr int NTHR = 576;
#define AUX_LD 16
#define AUX_ST 16
#define KPRE 2
#define VPRE 6
#define NTH 4
#define NTV NTH
#define WLATE 6
#define RDELAY 32
#define MAPV 2
#define QDELAY 8
constexpr u32 SPIN_CAP = 1u << 18;

constexpr int WS_E = 0;
constexpr int WS_QP = 4096;
constexpr int WS_GP = WS_QP + 32 * 4 * 2 * 128 * 8;
constexpr int OXS = 1152;
constexpr int WS_OX = WS_GP + 2 * 2 * 4 * DF * 8;
constexpr int WS_YP = WS_OX + 256 * OXS;
constexpr int WS_END = WS_YP + 4 * 5 * 32 * 128 * 8;
constexpr int OOB = 0x7ffffff0;

constexpr int L_XS = 0;
constexpr int L_R1 = L_XS + 8 * 160;
constexpr int L_QS = L_R1 + 2048;
constexpr int L_HS = L_QS + 64;
constexpr int L_OS = L_HS + 256;
constexpr int L_MISC = L_OS + 512;
constexpr int L_FLG = L_MISC + 8;
constexpr int L_ML = L_FLG + 28;
constexpr int L_REC = L_ML + 16;
constexpr int L_YR = L_REC + 1024;
constexpr int L_END = L_YR + 4096;

struct Params {
  const char* x; const char* k; const char* v; const char* wq; const char* wo;
  const char* wg; const char* wu; const char* wd; const char* gw; char* out; char* ws;
};

#define STAMP(i)
#define STAMPV(i, v)
#define STAMP_DECL
#define STAMP_OUT()
#define SB() __builtin_amdgcn_sched_barrier(0)
#define PIN8(a) asm volatile("" : "+v"(a[0]), "+v"(a[1]), "+v"(a[2]), "+v"(a[3]), "+v"(a[4]), "+v"(a[5]), "+v"(a[6]), "+v"(a[7]))
#define THR(n) do { SB(); __builtin_amdgcn_s_waitcnt(((n) & 15) | (7 << 4) | (15 << 8) | (((n) >> 4) << 14)); SB(); } while (0)
#define CLOB() asm volatile("" ::: "memory")
#define WG_BARRIER() do { asm volatile("s_waitcnt lgkmcnt(0)" ::: "memory"); __builtin_amdgcn_s_barrier(); CLOB(); } while (0)
#define XST __builtin_amdgcn_raw_buffer_store_b128
#define XLD4 __builtin_amdgcn_raw_buffer_load_b128
#define XLD2 __builtin_amdgcn_raw_buffer_load_b64

__device__ __forceinline__ u32x4 ldnt(const char* p) { return __builtin_nontemporal_load(reinterpret_cast<const u32x4*>(p)); }
__device__ __forceinline__ float asf(u32 x) { return __builtin_bit_cast(float, x); }
__device__ __forceinline__ u32 asu(float x) { return __builtin_bit_cast(u32, x); }
__device__ __forceinline__ float lo16(u32 x) { return asf(x << 16); }
__device__ __forceinline__ float hi16(u32 x) { return asf(x & 0xffff0000u); }
__device__ __forceinline__ float dot2(u32 a, u32 b, float acc) {
  return __builtin_amdgcn_fdot2_f32_bf16(__builtin_bit_cast(bf16x2, a), __builtin_bit_cast(bf16x2, b), acc, false);
}
__device__ __forceinline__ u32 pkbf(float lo, float hi) {
  bf16x2 r; r[0] = (__bf16)lo; r[1] = (__bf16)hi; return __builtin_bit_cast(u32, r);
}
__device__ __forceinline__ float rbf(float x) { return lo16(pkbf(x, 0.f)); }
template <int CTRL> __device__ __forceinline__ float dpp(float x) {
  return asf(__builtin_amdgcn_mov_dpp(__builtin_bit_cast(int, x), CTRL, 0xf, 0xf, true));
}
constexpr int XOR1 = 0xB1, XOR2 = 0x4E, XOR7 = 0x141, XOR8 = 0x128, BCAST = 0x150;
__device__ __forceinline__ float red32(float a, float b) {
  auto s = __builtin_amdgcn_permlane32_swap(asu(a), asu(b), false, false); return asf(s[0]) + asf(s[1]);
}
__device__ __forceinline__ float red16(float a, float b) {
  auto s = __builtin_amdgcn_permlane16_swap(asu(a), asu(b), false, false); return asf(s[0]) + asf(s[1]);
}
__device__ __forceinline__ float max32(float a) {
  auto s = __builtin_amdgcn_permlane32_swap(asu(a), asu(a), false, false); return fmaxf(asf(s[0]), asf(s[1]));
}
__device__ __forceinline__ float max16(float a) {
  auto s = __builtin_amdgcn_permlane16_swap(asu(a), asu(a), false, false); return fmaxf(asf(s[0]), asf(s[1]));
}
__device__ __forceinline__ float wave_sum(float s) {
  s += dpp<XOR1>(s); s += dpp<XOR2>(s); s += dpp<XOR7>(s); s += dpp<XOR8>(s); s = red16(s, s); return red32(s, s);
}
__device__ __forceinline__ float geglu(float g, float u) {
  const float ge = rbf(0.5f * g * (1.f + tanhf(0.7978845608028654f * (g + 0.044715f * g * g * g))));
  return ge * u;
}

__device__ __forceinline__ void pv_step(float (&acc)[8], float p, const u32x4& v) {
#pragma unroll
  for (int k = 0; k < 4; ++k) { acc[2 * k] = fmaf(p, lo16(v[k]), acc[2 * k]); acc[2 * k + 1] = fmaf(p, hi16(v[k]), acc[2 * k + 1]); }
}
__device__ __forceinline__ f32x4 gemv_mfma(f32x4 d, u32 mv, const u32x4& mk, const u32x4& w) {
  const u32x4 a = {mv & mk[0], mv & mk[1], mv & mk[2], mv & mk[3]};
  return __builtin_amdgcn_mfma_f32_16x16x32_bf16(__builtin_bit_cast(bf16x8, a), __builtin_bit_cast(bf16x8, w), d, 0, 0, 0);
}
template <int N, int NAP>
__device__ __forceinline__ u32 gather(const __amdgpu_buffer_rsrc_t rs, int (&off)[N], u32x4 (&g)[N], u32 tag) {
  u32 spins = 0;
#pragma unroll
  for (int u = 0; u < N; ++u) g[u] = u32x4{0u, 0u, 0u, 0u};
  for (;; ++spins) {
    CLOB();
    u32x4 n[N];
#pragma unroll
    for (int u = 0; u < N; ++u) n[u] = XLD4(rs, off[u], 0, AUX_LD);
    bool ok = true;
#pragma unroll
    for (int u = 0; u < N; ++u) { if (off[u] != OOB) { g[u] = n[u]; if ((n[u][1] == tag) & (n[u][3] == tag)) off[u] = OOB; } ok &= off[u] == OOB; }
    if (__all(ok) || spins > SPIN_CAP) break;
    __builtin_amdgcn_s_sleep(NAP);
  }
  return spins;
}
template <int I> struct PV {
  static __device__ __forceinline__ float bc(float p) { return dpp<BCAST + I>(p); }
};

typedef volatile __attribute__((address_space(3))) u32 vlds_u32;
__device__ __forceinline__ void lds_set(u32* f, int lane) { CLOB(); if (lane == 0) *(vlds_u32*)f = 1u; CLOB(); }
__device__ __forceinline__ void lds_wait(u32* f, int n, int lane) {
  vlds_u32* q = (vlds_u32*)(f + (lane < n ? lane : 0));
  for (u32 spins = 0; spins < (SPIN_CAP << 4); ++spins) { if (__all(*q != 0u)) break; __builtin_amdgcn_s_sleep(1); }
  CLOB();
}

__global__ void __launch_bounds__(NTHR, 3) fused_decode(Params p) {
  __shared__ __attribute__((aligned(16))) float lds[L_END];
  const int bid = blockIdx.x, tid = threadIdx.x, lane = tid & 63;
  const int wave = __builtin_amdgcn_readfirstlane(tid >> 6);
  const int xcd = bid & 7, slot = bid >> 3;
  const int t = slot & 7, h = (slot >> 3) | ((xcd ^ t) << 2);
  const int b = t >> 1, j = t & 1, wg = h * 8 + t;
  const bool isq = t >= 4;
  const int mt = t * 32 + h;
  const bool s1on = isq || mt < 108;
  const int mm = mt >= 54 ? 1 : 0, mc = (mt - 54 * mm) >> 1, rh = isq ? ((t >> 1) & 1) : (mt & 1);
  const bool wown = t < 5;
  const __amdgpu_buffer_rsrc_t rs = __builtin_amdgcn_make_buffer_rsrc((void*)p.ws, 0, WS_END, 0x00020000);
  u32* ldsu = reinterpret_cast<u32*>(lds);
  u32* flg = ldsu + L_FLG;
  STAMP_DECL;
  STAMP(0);

  if (__builtin_expect(wave == 8, 0)) {
    __builtin_amdgcn_s_setprio(3);
    const u32 ev = __builtin_amdgcn_raw_buffer_load_b32(rs, WS_E, 0, AUX_LD);
    u32x4 xv[5], gv[5];
#pragma unroll
    for (int i = 0; i < 5; ++i) {
      xv[i] = ldnt(p.x + (lane >> 4) * (DM * 2) + (i * 16 + (lane & 15)) * 16);
      gv[i] = ldnt(p.gw + (i * 16 + (lane & 15)) * 16);
    }
    if (lane < 28) flg[lane] = 0u;
    WG_BARRIER();
    const u32 tag = __builtin_amdgcn_readfirstlane(ev) + 1u;
    {
      float s = 0.f;
#pragma unroll
      for (int i = 0; i < 5; ++i)
#pragma unroll
        for (int k = 0; k < 4; ++k) s = dot2(xv[i][k], gv[i][k], s);
      s += dpp<XOR1>(s); s += dpp<XOR2>(s); s += dpp<XOR7>(s); s += dpp<XOR8>(s);
      if ((lane & 15) == 0) lds[L_MISC + (lane >> 4)] = 1.f / (1.f + __expf(-s));
      if (lane == 0) ldsu[L_MISC + 4] = tag;
    }
    lds_wait(flg + 0, 8, lane);
    STAMP(11);
    if (s1on) {
#pragma unroll
      for (int u = 0; u < 2; ++u) {
        const float* d = lds + L_R1 + u * 128 + lane * 2;
        f32x2 s = *reinterpret_cast<const f32x2*>(d);
#pragma unroll
        for (int w = 1; w < 8; ++w) s += *reinterpret_cast<const f32x2*>(d + w * 256);
        const int bb = 2 * u + (lane >> 5);
        const int off = isq ? WS_QP + ((h * 4 + bb) * 2 + rh) * 1024 + (t & 1) * 512 : WS_GP + (((mm * 2 + rh) * 4 + bb) * DF + mc * 64) * 8;
        XST(u32x4{asu(s[0]), tag, asu(s[1]), tag}, rs, off + (lane & 31) * 16, 0, AUX_ST);
      }
    }
    STAMP(1);
    __builtin_amdgcn_s_sleep(QDELAY);
    {
      u32x4 g[2];
      int off[2];
#pragma unroll
      for (int u = 0; u < 2; ++u) off[u] = WS_QP + ((h * 4 + b) * 2 + u) * 1024 + lane * 16;
      const u32 sp_ = gather<2, 2>(rs, off, g, tag);
      STAMPV(12, sp_); (void)sp_;
      ldsu[L_QS + lane] = pkbf(asf(g[0][0]) + asf(g[1][0]), asf(g[0][2]) + asf(g[1][2]));
      lds_set(flg + 24, lane);
      STAMP(7);
    }
    if (wown) {
      const int i = lane & 31, m = lane >> 5;
      u32x4 g[8];
      int off[8];
#pragma unroll
      for (int u = 0; u < 8; ++u) off[u] = i < 27 ? WS_GP + ((m * 8 + u) * DF + 54 * h + 2 * i) * 8 : OOB;
      gather<8, 8>(rs, off, g, tag);
#pragma unroll
      for (int bb = 0; bb < 4; ++bb) {
        const float a0 = rbf(asf(g[bb][0]) + asf(g[4 + bb][0])), a1 = rbf(asf(g[bb][2]) + asf(g[4 + bb][2]));
        auto s = __builtin_amdgcn_permlane32_swap(asu(a0), asu(a1), false, false);
        const float hv = geglu(asf(s[0]), asf(s[1]));
        ldsu[L_HS + bb * 64 + 2 * i + m] = pkbf(hv, hv);
      }
      lds_set(flg + 25, lane);
    }
    STAMP(8);
    float fw[8], M, L;
    lds_wait(flg + 8, 8, lane);
    {
      f32x4 ml[4];
#pragma unroll
      for (int u = 0; u < 4; ++u) ml[u] = *reinterpret_cast<const f32x4*>(lds + L_ML + 4 * u);
      M = fmaxf(fmaxf(fmaxf(ml[0][0], ml[0][2]), fmaxf(ml[1][0], ml[1][2])), fmaxf(fmaxf(ml[2][0], ml[2][2]), fmaxf(ml[3][0], ml[3][2])));
      L = 0.f;
#pragma unroll
      for (int w = 0; w < 8; ++w) { fw[w] = __builtin_amdgcn_exp2f(ml[w >> 1][(w & 1) * 2] - M); L = fmaf(fw[w], ml[w >> 1][(w & 1) * 2 + 1], L); }
      if (lane == 0) XST(u32x4{asu(M), tag, asu(L), tag}, rs, WS_OX + wg * OXS + 1024, 0, AUX_ST);
    }
    STAMP(2);
    float sa, sb;
    {
      u32x4 g;
      for (u32 spins = 0;; ++spins) {
        CLOB();
        g = XLD4(rs, lane == 0 ? WS_OX + (wg ^ 1) * OXS + 1024 : OOB, 0, AUX_LD);
#pragma unroll
        for (int k = 0; k < 4; ++k) g[k] = __builtin_amdgcn_readfirstlane(g[k]);
        if (((g[1] == tag) & (g[3] == tag)) || spins > SPIN_CAP) { STAMPV(16, spins); break; }
        __builtin_amdgcn_s_sleep(16);
      }
      const float M2 = asf(g[0]), L2 = asf(g[2]), MM = fmaxf(M, M2);
      sa = __builtin_amdgcn_exp2f(M - MM); sb = __builtin_amdgcn_exp2f(M2 - MM);
      const float inv = 1.f / (sa * L + sb * L2);
      sa *= inv; sb *= inv; (void)sb;
#pragma unroll
      for (int w = 0; w < 8; ++w) fw[w] *= sa;
    }
    STAMP(3);
    lds_wait(flg + 16, 8, lane);
    {
      f32x2 o = {0.f, 0.f};
#pragma unroll
      for (int w = 0; w < 8; ++w) { const f32x2 r = *reinterpret_cast<const f32x2*>(lds + L_REC + w * 128 + lane * 2); o[0] = fmaf(fw[w], r[0], o[0]); o[1] = fmaf(fw[w], r[1], o[1]); }
      XST(u32x4{asu(o[0]), tag, asu(o[1]), tag}, rs, WS_OX + wg * OXS + lane * 16, 0, AUX_ST);
      STAMP(14);
    }
    if (wown) {
      u32x4 g[8];
      int off[8];
#pragma unroll
      for (int u = 0; u < 8; ++u) off[u] = WS_OX + (h * 8 + u) * OXS + lane * 16;
      const u32 sp_ = gather<8, 1>(rs, off, g, tag);
      STAMPV(17, sp_); (void)sp_;
#pragma unroll
      for (int bb = 0; bb < 4; ++bb) {
        const float o0 = asf(g[2 * bb][0]) + asf(g[2 * bb + 1][0]), o1 = asf(g[2 * bb][2]) + asf(g[2 * bb + 1][2]);
        *reinterpret_cast<u32x2*>(ldsu + L_OS + bb * 128 + 2 * lane) = u32x2{pkbf(o0, o0), pkbf(o1, o1)};
      }
      lds_set(flg + 26, lane);
      STAMP(10);
      WG_BARRIER();
    } else {
      const int r = h * 3 + t - 5;
      if (r < 80) {
        const int rb = r / 20, rc = r - 20 * rb;
        u32x4 g[8];
        int off[8];
#pragma unroll
        for (int u = 0; u < 8; ++u) off[u] = WS_YP + ((rb * 5 + (rc >> 2)) * 32 + u * 4 + (lane >> 4)) * 1024 + (rc & 3) * 256 + (lane & 15) * 16;
        __builtin_amdgcn_s_sleep(RDELAY);
        gather<8, 1>(rs, off, g, tag);
        float y0 = 0.f, y1 = 0.f;
#pragma unroll
        for (int u = 0; u < 8; ++u) { y0 += asf(g[u][0]); y1 += asf(g[u][2]); }
        y0 = red16(y0, y0); y1 = red16(y1, y1);
        y0 = red32(y0, y0); y1 = red32(y1, y1);
        if (lane < 16) *reinterpret_cast<u32*>(p.out + (rb * DM + 32 * rc + 2 * lane) * 2) = pkbf(y0, y1);
        STAMP(9);
      } else if (wg == 255) {
        u32x4 g[4];
        int off[4];
#pragma unroll
        for (int u = 0; u < 4; ++u) off[u] = WS_OX + (u * 64 + lane) * OXS + 1024;
        gather<4, 4>(rs, off, g, tag);
        if (lane == 0) __builtin_amdgcn_raw_buffer_store_b32(tag, rs, WS_E, 0, AUX_ST);
      }
    }
    STAMP_OUT();
    return;
  }

  const int row0 = 320 * rh + 40 * wave;
  u32x4 S1[5], K[16], V[16], W[6];
  const int xb = min(lane / 10, 3), xc = min(lane - 10 * xb, 9);
  const u32x2 xr = __builtin_nontemporal_load(reinterpret_cast<const u32x2*>(p.x + xb * (DM * 2) + (row0 + 4 * xc) * 2));
  {
    const char* sp; int sstep;
    if (isq) { sp = p.wq + (size_t)(row0 + (lane >> 3)) * 8192 + h * 256 + (t & 1) * 128 + (lane & 7) * 16; sstep = 8 * 8192; }
    else { sp = (mm ? p.wu : p.wg) + (size_t)(row0 + (lane >> 3)) * (DF * 2) + mc * 128 + (lane & 7) * 16; sstep = 8 * DF * 2; }
    if (!s1on) { sp = p.x + (lane & 15) * 16; sstep = 0; }
#pragma unroll
    for (int i = 0; i < 5; ++i) S1[i] = ldnt(sp + i * sstep);
  }
  const size_t kvoff = ((size_t)(b * NH + h) * SK + (SK - WIN) + j * 512 + wave * 64) * 256 + lane * 16;
  const char* kp = p.k + kvoff;
  const char* vp = p.v + kvoff;
  SB();
#pragma unroll
  for (int i = 0; i < KPRE; ++i) K[i] = ldnt(kp + i * 1024);
  SB();
  WG_BARRIER();
  {
    float* xs = lds + L_XS + wave * 160;
#pragma unroll
    for (int k = 0; k < 2; ++k) { xs[(4 * xc + 2 * k) * 4 + xb] = lo16(xr[k]); xs[(4 * xc + 2 * k + 1) * 4 + xb] = hi16(xr[k]); }
    asm volatile("s_waitcnt lgkmcnt(0)" ::: "memory");
    float a[4][8];
#pragma unroll
    for (int bb = 0; bb < 4; ++bb)
#pragma unroll
      for (int k = 0; k < 8; ++k) a[bb][k] = 0.f;
#pragma unroll
    for (int i = 0; i < 5; ++i) {
      const f32x4 xv = *reinterpret_cast<const f32x4*>(xs + ((lane >> 3) + i * 8) * 4);
#pragma unroll
      for (int bb = 0; bb < 4; ++bb) pv_step(a[bb], xv[bb], S1[i]);
    }
    float r[8];
#pragma unroll
    for (int k = 0; k < 8; ++k) {
      r[k] = red16(red32(a[0][k], a[2][k]), red32(a[1][k], a[3][k]));
      r[k] += dpp<XOR8>(r[k]);
    }
    float* d = lds + L_R1 + wave * 256 + (lane >> 4) * 64 + (lane & 7) * 8 + ((lane & 8) >> 1);
    *reinterpret_cast<f32x4*>(d) = (lane & 8) ? f32x4{r[4], r[5], r[6], r[7]} : f32x4{r[0], r[1], r[2], r[3]};
    lds_set(flg + wave, lane);
  }
  SB();
  const __amdgpu_buffer_rsrc_t rd = __builtin_amdgcn_make_buffer_rsrc((void*)(p.wd + (size_t)(54 * h) * (DM * 2)), 0, wown ? 54 * DM * 2 : 0, 0x00020000);
  const __amdgpu_buffer_rsrc_t ro = __builtin_amdgcn_make_buffer_rsrc((void*)(p.wo + (size_t)(128 * h) * (DM * 2)), 0, wown ? 128 * DM * 2 : 0, 0x00020000);
  const int wdo = (8 * wave + 2 * (lane >> 4)) * (DM * 2) + 256 * t + (lane & 15) * 16;
  const int woo = (16 * wave + 4 * (lane >> 4)) * (DM * 2) + 256 * t + (lane & 15) * 16;
#define CL(n_, lo_, hi_) ((n_) >= (lo_) && (n_) < (hi_) ? (n_) - (lo_) : 0)
#define SLOAD(n_) do { \
    if ((n_) < 16) V[CL(n_, 0, 16)] = ldnt(vp + (n_) * 1024); \
    else if ((n_) < 18) W[CL(n_, 16, 22)] = __builtin_amdgcn_raw_buffer_load_b128(rd, wdo + ((n_) - 16) * (DM * 2), 0, 2); \
    else if ((n_) < 22) W[CL(n_, 16, 22)] = __builtin_amdgcn_raw_buffer_load_b128(ro, woo + ((n_) - 18) * (DM * 2), 0, 2); \
  } while (0)
#pragma unroll
  for (int i = KPRE; i < 16; ++i) { THR(NTH - 1); K[i] = ldnt(kp + i * 1024); }
  STAMP(13);
#pragma unroll
  for (int i = 0; i < VPRE; ++i) { THR(NTH - 1); SLOAD(i); }
  SB();
  lds_wait(flg + 24, 1, lane);
  float pe;
  {
    const u32x4 q = *reinterpret_cast<const u32x4*>(ldsu + L_QS + (lane & 15) * 4);
    float x[16];
#pragma unroll
    for (int i = 0; i < 16; ++i) { float d = 0.f;
#pragma unroll
      for (int k = 0; k < 4; ++k) d = dot2(K[i][k], q[k], d);
      x[i] = d; }
    SB();
    SLOAD(VPRE);
    SB();
#define TR_STEP(HALF, CTRL) _Pragma("unroll") for (int u = 0; u < (HALF); ++u) { \
      const float t1_ = x[u] + dpp<CTRL>(x[u]), t2_ = x[u + (HALF)] + dpp<CTRL>(x[u + (HALF)]); x[u] = (lane & (HALF)) ? t2_ : t1_; }
    TR_STEP(8, XOR8) TR_STEP(4, XOR7) TR_STEP(2, XOR2) TR_STEP(1, XOR1)
#undef TR_STEP
    const float s = x[0] * (0.08838834764831845f * 1.4426950408889634f);
    float m = s;
    m = fmaxf(m, dpp<XOR1>(m)); m = fmaxf(m, dpp<XOR2>(m)); m = fmaxf(m, dpp<XOR7>(m)); m = fmaxf(m, dpp<XOR8>(m)); m = max16(m); m = max32(m);
    pe = __builtin_amdgcn_exp2f(s - m);
    const float l = wave_sum(pe);
    if (lane == 0) *reinterpret_cast<f32x2*>(lds + L_ML + 2 * wave) = f32x2{m, l};
    lds_set(flg + 8 + wave, lane);
  }
  STAMP(4);
  SB();
  float acc[8];
#pragma unroll
  for (int k = 0; k < 8; ++k) acc[k] = 0.f;
  constexpr int WE = 6 - WLATE;
#define SEQ(n_) ((n_) <= VPRE ? (n_) : (n_) <= VPRE + WE ? (n_) + 15 - VPRE : (n_) <= 15 + WE ? (n_) - WE : (n_))
#define MIN22(a_) ((a_) < 22 ? (a_) : 22)
#define ISS(i_) ((i_) > VPRE ? MIN22((i_) + WE + NTV) : VPRE + 1 + ((i_) * (WE + NTV) + VPRE) / (VPRE + 1))
#define PVS(i_) do { _Pragma("unroll") for (int n_ = ISS(i_); n_ < ISS((i_) + 1); ++n_) { THR(NTV - 1); SLOAD(SEQ(n_)); } \
    SB(); pv_step(acc, PV<i_>::bc(pe), V[i_]); PIN8(acc); SB(); } while (0)
  PVS(0); PVS(1); PVS(2); PVS(3); PVS(4); PVS(5); PVS(6); PVS(7);
  PVS(8); PVS(9); PVS(10); PVS(11); PVS(12); PVS(13); PVS(14); PVS(15);
#pragma unroll
  for (int i = ISS(16); i < 22; ++i) SLOAD(SEQ(i));
  SB();
  {
    float u4[4];
#pragma unroll
    for (int k = 0; k < 4; ++k) u4[k] = red32(acc[k], acc[k + 4]);
    *reinterpret_cast<f32x2*>(lds + L_REC + wave * 128 + 8 * (lane & 15) + 2 * (lane >> 4)) = f32x2{red16(u4[0], u4[2]), red16(u4[1], u4[3])};
    lds_set(flg + 16 + wave, lane);
  }
  STAMP(5);
  SB();
  if (!wown) { STAMP_OUT(); return; }
  u32x4 mk;
#pragma unroll
  for (int k = 0; k < 4; ++k) mk[k] = ((lane >> 1) & 3) == k ? ((lane & 1) ? 0xffff0000u : 0xffffu) : 0u;
  f32x4 dm[2], da[2];
  lds_wait(flg + 25, 1, lane);
  const u32 tag = ldsu[L_MISC + 4];
  float gate[2];
#pragma unroll
  for (int u = 0; u < 2; ++u) {
    gate[u] = lds[L_MISC + 2 * u + (lane >> 5)];
    const u32x2 hv = *reinterpret_cast<const u32x2*>(ldsu + L_HS + (2 * u + ((lane >> 3) & 1)) * 64 + 8 * wave + 2 * (lane >> 4));
    dm[u] = gemv_mfma(f32x4{0.f, 0.f, 0.f, 0.f}, hv[0], mk, W[0]);
    dm[u] = gemv_mfma(dm[u], hv[1], mk, W[1]);
    dm[u] *= (1.f - gate[u]);
  }
  asm volatile("" : "+v"(dm[0]), "+v"(dm[1]));
  SB();
  lds_wait(flg + 26, 1, lane);
  {
    u32x4 ov[2];
#pragma unroll
    for (int u = 0; u < 2; ++u) { ov[u] = *reinterpret_cast<const u32x4*>(ldsu + L_OS + (2 * u + ((lane >> 3) & 1)) * 128 + 16 * wave + 4 * (lane >> 4)); da[u] = f32x4{0.f, 0.f, 0.f, 0.f}; }
#pragma unroll
    for (int n = 0; n < 4; ++n)
#pragma unroll
      for (int u = 0; u < 2; ++u) da[u] = gemv_mfma(da[u], ov[u][n], mk, W[2 + n]);
#pragma unroll
    for (int u = 0; u < 2; ++u)
      *reinterpret_cast<f32x4*>(lds + L_YR + wave * 512 + (2 * u + (lane >> 5)) * 128 + 8 * (lane & 15) + 4 * ((lane >> 4) & 1)) = dm[u] + gate[u] * da[u];
  }
  WG_BARRIER();
  STAMP(6);
  if (wave < 4) {
    const float* y = lds + L_YR + tid * 2;
    f32x2 s = *reinterpret_cast<const f32x2*>(y);
#pragma unroll
    for (int w = 1; w < 8; ++w) s += *reinterpret_cast<const f32x2*>(y + w * 512);
    XST(u32x4{asu(s[0]), tag, asu(s[1]), tag}, rs, WS_YP + ((wave * 5 + t) * 32 + h) * 1024 + lane * 16, 0, AUX_ST);
  }
  STAMP_OUT();
}

extern "C" void kernel_launch(void* const* d_in, const int* in_sizes, int n_in, void* d_out, int out_size, void* d_ws, size_t ws_size,
                              hipStream_t stream) {
  Params p;
  p.x = (const char*)d_in[0]; p.k = (const char*)d_in[1]; p.v = (const char*)d_in[2]; p.wq = (const char*)d_in[3]; p.wo = (const char*)d_in[4];
  p.wg = (const char*)d_in[6]; p.wu = (const char*)d_in[7]; p.wd = (const char*)d_in[8]; p.gw = (const char*)d_in[9];
  p.out = (char*)d_out; p.ws = (char*)d_ws;
  hipLaunchKernelGGL(fused_decode, dim3(256), dim3(NTHR), 0, stream, p);
}
```

```cpp
#include <hip/hip_runtime.h>
#include <hip/hip_bf16.h>
#include <stdint.h>

typedef unsigned u32;
using u32x4 = __attribute__((ext_vector_type(4))) unsigned;
using u32x2 = __attribute__((ext_vector_type(2))) unsigned;
using f32x4 = __attribute__((ext_vector_type(4))) float;
using f32x2 = __attribute__((ext_vector_type(2))) float;
using bf16x2 = __attribute__((ext_vector_type(2))) __bf16;
using bf16x8 = __attribute__((ext_vector_type(8))) __bf16;
using s16x4 = __attribute__((ext_vector_type(4))) short;

constexpr int DM = 640, NH = 32, SK = 4096, WIN = 1024, DF = 1728;
constexpr int NTHR = 576;
#define AUX_LD 16
#define AUX_ST 16
#define KPRE 0
#define KMID 4
#define VPRE 6
#define NTH 4
#define NTV NTH
#define WLATE 6
#define RNAP 1
#define RDELAY 32
#define MAPV 2
#define QDELAY 16
constexpr u32 SPIN_CAP = 1u << 18;

constexpr int WS_E = 0;
constexpr int WS_QP = 4096;
constexpr int WS_GP = WS_QP + 32 * 4 * 2 * 128 * 8;
constexpr int OXS = 1152;
constexpr int WS_OX = WS_GP + 2 * 2 * 4 * DF * 8;
constexpr int WS_YP = WS_OX + 256 * OXS;
constexpr int WS_END = WS_YP + 4 * 5 * 32 * 128 * 8;
constexpr int OOB = 0x7ffffff0;

constexpr int L_XS = 0;
constexpr int L_R1 = L_XS + 8 * 160;
constexpr int L_QS = L_R1 + 2048;
constexpr int L_HS = L_QS + 64;
constexpr int L_OS = L_HS + 128;
constexpr int L_MISC = L_OS + 256;
constexpr int L_FLG = L_MISC + 8;
constexpr int L_ML = L_FLG + 36;
constexpr int L_REC = L_ML + 16;
constexpr int WROW = 72;
constexpr int L_W = L_REC + 1024;
constexpr int L_END = L_W + 192 * WROW;

struct Params {
  const char* x; const char* k; const char* v; const char* wq; const char* wo;
  const char* wg; const char* wu; const char* wd; const char* gw; char* out; char* ws;
};

#define STAMP(i)
#define STAMPV(i, v)
#define STAMP_DECL
#define STAMP_OUT()
#define SB() __builtin_amdgcn_sched_barrier(0)
#define PIN8(a) asm volatile("" : "+v"(a[0]), "+v"(a[1]), "+v"(a[2]), "+v"(a[3]), "+v"(a[4]), "+v"(a[5]), "+v"(a[6]), "+v"(a[7]))
#define THR(n) do { SB(); __builtin_amdgcn_s_waitcnt(((n) & 15) | (7 << 4) | (15 << 8) | (((n) >> 4) << 14)); SB(); } while (0)
#define CLOB() asm volatile("" ::: "memory")
#define WG_BARRIER() do { asm volatile("s_waitcnt lgkmcnt(0)" ::: "memory"); __builtin_amdgcn_s_barrier(); CLOB(); } while (0)
#define XST __builtin_amdgcn_raw_buffer_store_b128
#define XST2 __builtin_amdgcn_raw_buffer_store_b64
#define XLD4 __builtin_amdgcn_raw_buffer_load_b128

__device__ __forceinline__ u32x4 ldnt(const char* p) { return __builtin_nontemporal_load(reinterpret_cast<const u32x4*>(p)); }
__device__ __forceinline__ float asf(u32 x) { return __builtin_bit_cast(float, x); }
__device__ __forceinline__ u32 asu(float x) { return __builtin_bit_cast(u32, x); }
__device__ __forceinline__ float lo16(u32 x) { return asf(x << 16); }
__device__ __forceinline__ float hi16(u32 x) { return asf(x & 0xffff0000u); }
__device__ __forceinline__ float dot2(u32 a, u32 b, float acc) {
  return __builtin_amdgcn_fdot2_f32_bf16(__builtin_bit_cast(bf16x2, a), __builtin_bit_cast(bf16x2, b), acc, false);
}
__device__ __forceinline__ u32 pkbf(float lo, float hi) {
  bf16x2 r; r[0] = (__bf16)lo; r[1] = (__bf16)hi; return __builtin_bit_cast(u32, r);
}
__device__ __forceinline__ float rbf(float x) { return lo16(pkbf(x, 0.f)); }
template <int CTRL> __device__ __forceinline__ float dpp(float x) {
  return asf(__builtin_amdgcn_mov_dpp(__builtin_bit_cast(int, x), CTRL, 0xf, 0xf, true));
}
constexpr int XOR1 = 0xB1, XOR2 = 0x4E, XOR7 = 0x141, XOR8 = 0x128, BCAST = 0x150;
__device__ __forceinline__ float red32(float a, float b) {
  auto s = __builtin_amdgcn_permlane32_swap(asu(a), asu(b), false, false); return asf(s[0]) + asf(s[1]);
}
__device__ __forceinline__ float red16(float a, float b) {
  auto s = __builtin_amdgcn_permlane16_swap(asu(a), asu(b), false, false); return asf(s[0]) + asf(s[1]);
}
__device__ __forceinline__ float max32(float a) {
  auto s = __builtin_amdgcn_permlane32_swap(asu(a), asu(a), false, false); return fmaxf(asf(s[0]), asf(s[1]));
}
__device__ __forceinline__ float max16(float a) {
  auto s = __builtin_amdgcn_permlane16_swap(asu(a), asu(a), false, false); return fmaxf(asf(s[0]), asf(s[1]));
}
__device__ __forceinline__ float wave_sum(float s) {
  s += dpp<XOR1>(s); s += dpp<XOR2>(s); s += dpp<XOR7>(s); s += dpp<XOR8>(s); s = red16(s, s); return red32(s, s);
}
__device__ __forceinline__ float geglu(float g, float u) {
  const float ge = rbf(0.5f * g * (1.f + tanhf(0.7978845608028654f * (g + 0.044715f * g * g * g))));
  return ge * u;
}

__device__ __forceinline__ void pv_step(float (&acc)[8], float p, const u32x4& v) {
#pragma unroll
  for (int k = 0; k < 4; ++k) { acc[2 * k] = fmaf(p, lo16(v[k]), acc[2 * k]); acc[2 * k + 1] = fmaf(p, hi16(v[k]), acc[2 * k + 1]); }
}
__device__ __forceinline__ f32x4 gemv_mfma(f32x4 d, u32 mv, const u32x4& mk, const u32x4& w) {
  const u32x4 a = {mv & mk[0], mv & mk[1], mv & mk[2], mv & mk[3]};
  return __builtin_amdgcn_mfma_f32_16x16x32_bf16(__builtin_bit_cast(bf16x8, a), __builtin_bit_cast(bf16x8, w), d, 0, 0, 0);
}
__device__ __forceinline__ f32x4 mfma(const u32x4& a, const u32x4& b, f32x4 d) {
  return __builtin_amdgcn_mfma_f32_16x16x32_bf16(__builtin_bit_cast(bf16x8, a), __builtin_bit_cast(bf16x8, b), d, 0, 0, 0);
}
__device__ __forceinline__ u32x2 lds_tr(const u32* p) {
  return __builtin_bit_cast(u32x2, __builtin_amdgcn_ds_read_tr16_b64_v4i16((__attribute__((address_space(3))) s16x4*)p));
}
template <int N, int NAP>
__device__ __forceinline__ u32 gather(const __amdgpu_buffer_rsrc_t rs, int (&off)[N], u32x4 (&g)[N], u32 tag) {
  u32 spins = 0;
#pragma unroll 1
  for (;; ++spins) {
    CLOB();
    u32x4 n[N];
#pragma unroll
    for (int u = 0; u < N; ++u) n[u] = XLD4(rs, off[u], 0, AUX_LD);
    bool ok = true;
#pragma unroll
    for (int u = 0; u < N; ++u) {
      const bool live = off[u] != OOB, hit = (n[u][1] == tag) & (n[u][3] == tag);
      g[u][0] = live ? n[u][0] : g[u][0]; g[u][2] = live ? n[u][2] : g[u][2];
      off[u] = hit ? OOB : off[u];
      ok &= hit | !live;
    }
    if (__all(ok) || spins > SPIN_CAP) break;
    __builtin_amdgcn_s_sleep(NAP);
  }
  return spins;
}
template <int I> struct PV {
  static __device__ __forceinline__ float bc(float p) { return dpp<BCAST + I>(p); }
};

typedef volatile __attribute__((address_space(3))) u32 vlds_u32;
__device__ __forceinline__ void lds_set(u32* f, int lane) { CLOB(); if (lane == 0) *(vlds_u32*)f = 1u; CLOB(); }
template <int NAP>
__device__ __forceinline__ void lds_wait(u32* f, int n, int lane) {
  vlds_u32* q = (vlds_u32*)(f + (lane < n ? lane : 0));
#pragma unroll 1
  for (u32 spins = 0; spins < (SPIN_CAP << 4); ++spins) { if (__all(*q != 0u)) break; if (NAP) __builtin_amdgcn_s_sleep(NAP); }
  CLOB();
}

__global__ void __launch_bounds__(NTHR, 3) fused_decode(Params p) {
  __shared__ __attribute__((aligned(16))) float lds[L_END];
  const int bid = blockIdx.x, tid = threadIdx.x, lane = tid & 63;
  const int wave = __builtin_amdgcn_readfirstlane(tid >> 6);
  const int xcd = bid & 7, slot = bid >> 3;
  const int t = slot & 7, h = (slot >> 3) | ((xcd ^ t) << 2);
  const int b = t >> 1, j = t & 1, wg = h * 8 + t;
  const bool isq = t >= 4;
  const int mt = t * 32 + h;
  const bool s1on = isq || mt < 108;
  const int mm = mt >= 54 ? 1 : 0, mc = (mt - 54 * mm) >> 1, rh = isq ? ((t >> 1) & 1) : (mt & 1);
  const bool wown = t < 5;
  const __amdgpu_buffer_rsrc_t rs = __builtin_amdgcn_make_buffer_rsrc((void*)p.ws, 0, WS_END, 0x00020000);
  u32* ldsu = reinterpret_cast<u32*>(lds);
  u32* flg = ldsu + L_FLG;
  STAMP_DECL;
  STAMP(0);

  const int row0 = 320 * rh + 40 * (wave & 7);
  u32x4 S1[5], K[16], V[16], W[6];
  u32x2 xr = {0u, 0u};
  u32 ev = 0u;
  const int xb = min(lane / 10, 3), xc = min(lane - 10 * xb, 9);
  const size_t kvoff = ((size_t)(b * NH + h) * SK + (SK - WIN) + j * 512 + (wave & 7) * 64) * 256 + lane * 16;
  const char* kp = p.k + kvoff;
  const char* vp = p.v + kvoff;
  if (wave < 8) {
    xr = *reinterpret_cast<const u32x2*>(p.x + xb * (DM * 2) + (row0 + 4 * xc) * 2);
    const char* sp; int sstep;
    if (isq) { sp = p.wq + (size_t)(row0 + (lane >> 3)) * 8192 + h * 256 + (t & 1) * 128 + (lane & 7) * 16; sstep = 8 * 8192; }
    else { sp = (mm ? p.wu : p.wg) + (size_t)(row0 + (lane >> 3)) * (DF * 2) + mc * 128 + (lane & 7) * 16; sstep = 8 * DF * 2; }
    if (!s1on) { sp = p.x + (lane & 15) * 16; sstep = 0; }
#pragma unroll
    for (int i = 0; i < 5; ++i) S1[i] = ldnt(sp + i * sstep);
    SB();
#pragma unroll
    for (int i = 0; i < KPRE; ++i) K[i] = ldnt(kp + i * 1024);
    STAMP(18);
  } else {
    ev = __builtin_amdgcn_raw_buffer_load_b32(rs, WS_E, 0, 0);
    if (lane < 36) flg[lane] = 0u;
  }
  SB();
  WG_BARRIER();
  SB();
  STAMP(20);

  if (__builtin_expect(wave == 8, 0)) {
    __builtin_amdgcn_s_setprio(3);
    const u32 tag = __builtin_amdgcn_readfirstlane(ev) + 1u;
    lds_wait<1>(flg + 0, 8, lane);
    STAMP(11);
    if (s1on) {
#pragma unroll
      for (int u = 0; u < 2; ++u) {
        const float* d = lds + L_R1 + u * 128 + lane * 2;
        f32x2 s = *reinterpret_cast<const f32x2*>(d);
#pragma unroll
        for (int w = 1; w < 8; ++w) s += *reinterpret_cast<const f32x2*>(d + w * 256);
        const int bb = 2 * u + (lane >> 5);
        const int off = isq ? WS_QP + ((h * 4 + bb) * 2 + rh) * 1024 + (t & 1) * 512 : WS_GP + (((mm * 2 + rh) * 4 + bb) * DF + mc * 64) * 8;
        XST(u32x4{asu(s[0]), tag, asu(s[1]), tag}, rs, off + (lane & 31) * 16, 0, AUX_ST);
      }
    }
    STAMP(1);
    __builtin_amdgcn_s_sleep(QDELAY);
    {
      u32x4 g[2];
      int off[2];
#pragma unroll
      for (int u = 0; u < 2; ++u) { off[u] = WS_QP + ((h * 4 + b) * 2 + u) * 1024 + lane * 16; g[u] = u32x4{0u, 0u, 0u, 0u}; }
      const u32 sp_ = gather<2, 2>(rs, off, g, tag);
      STAMPV(12, sp_); (void)sp_;
      ldsu[L_QS + lane] = pkbf(asf(g[0][0]) + asf(g[1][0]), asf(g[0][2]) + asf(g[1][2]));
      lds_set(flg + 24, lane);
      STAMP(7);
    }
    if (wown) {
      const int i = lane & 31, m = lane >> 5;
      {
        float s = 0.f;
#pragma unroll
        for (int c = 0; c < 5; ++c) {
          const u32x4 xv = *reinterpret_cast<const u32x4*>(p.x + (lane >> 4) * (DM * 2) + (c * 16 + (lane & 15)) * 16);
          const u32x4 gv = *reinterpret_cast<const u32x4*>(p.gw + (c * 16 + (lane & 15)) * 16);
#pragma unroll
          for (int k = 0; k < 4; ++k) s = dot2(xv[k], gv[k], s);
        }
        s += dpp<XOR1>(s); s += dpp<XOR2>(s); s += dpp<XOR7>(s); s += dpp<XOR8>(s);
        if ((lane & 15) == 0) lds[L_MISC + (lane >> 4)] = 1.f / (1.f + __expf(-s));
        if (lane == 0) ldsu[L_MISC + 4] = tag;
      }
      u32x4 g[8];
      int off[8];
#pragma unroll
      for (int u = 0; u < 8; ++u) { off[u] = i < 27 ? WS_GP + ((m * 8 + u) * DF + 54 * h + 2 * i) * 8 : OOB; g[u] = u32x4{0u, 0u, 0u, 0u}; }
      gather<8, 8>(rs, off, g, tag);
#pragma unroll
      for (int bb = 0; bb < 4; ++bb) {
        const float a0 = rbf(asf(g[bb][0]) + asf(g[4 + bb][0])), a1 = rbf(asf(g[bb][2]) + asf(g[4 + bb][2]));
        auto s = __builtin_amdgcn_permlane32_swap(asu(a0), asu(a1), false, false);
        const float hv = geglu(asf(s[0]), asf(s[1]));
        reinterpret_cast<unsigned short*>(ldsu + L_HS)[bb * 64 + 2 * i + m] = (unsigned short)pkbf(hv, 0.f);
      }
      lds_set(flg + 25, lane);
    }
    STAMP(8);
    float fw[8], M, L;
    lds_wait<1>(flg + 8, 8, lane);
    {
      f32x4 ml[4];
#pragma unroll
      for (int u = 0; u < 4; ++u) ml[u] = *reinterpret_cast<const f32x4*>(lds + L_ML + 4 * u);
      M = fmaxf(fmaxf(fmaxf(ml[0][0], ml[0][2]), fmaxf(ml[1][0], ml[1][2])), fmaxf(fmaxf(ml[2][0], ml[2][2]), fmaxf(ml[3][0], ml[3][2])));
      L = 0.f;
#pragma unroll
      for (int w = 0; w < 8; ++w) { fw[w] = __builtin_amdgcn_exp2f(ml[w >> 1][(w & 1) * 2] - M); L = fmaf(fw[w], ml[w >> 1][(w & 1) * 2 + 1], L); }
    }
    STAMP(2);
    lds_wait<0>(flg + 16, 8, lane);
    f32x2 o = {0.f, 0.f};
    {
#pragma unroll
      for (int w = 0; w < 8; ++w) { const f32x2 r = *reinterpret_cast<const f32x2*>(lds + L_REC + w * 128 + lane * 2); o[0] = fmaf(fw[w], r[0], o[0]); o[1] = fmaf(fw[w], r[1], o[1]); }
      XST(u32x4{asu(o[0]), tag, asu(o[1]), tag}, rs, WS_OX + wg * OXS + lane * 16, 0, AUX_ST);
      if (lane == 0) XST(u32x4{asu(M), tag, asu(L), tag}, rs, WS_OX + wg * OXS + 1024, 0, AUX_ST);
      STAMP(14);
    }
    if (wown) {
      u32x4 g[9];
      int off[9];
#pragma unroll
      for (int u = 0; u < 8; ++u) { off[u] = u == t ? OOB : WS_OX + (h * 8 + u) * OXS + lane * 16; g[u] = u32x4{asu(o[0]), 0u, asu(o[1]), 0u}; }
      off[8] = (lane < 8 && lane != t) ? WS_OX + (h * 8 + lane) * OXS + 1024 : OOB; g[8] = u32x4{asu(M), 0u, asu(L), 0u};
      const u32 sp_ = gather<9, 1>(rs, off, g, tag);
      STAMPV(17, sp_); (void)sp_;
      const float mu = asf(g[8][0]);
      float sc = __builtin_amdgcn_exp2f(mu - fmaxf(mu, dpp<XOR1>(mu)));
      const float sl = sc * asf(g[8][2]);
      sc = sc / (sl + dpp<XOR1>(sl));
#pragma unroll
      for (int bb = 0; bb < 4; ++bb) {
        const float c0 = asf(__builtin_amdgcn_readlane(asu(sc), 2 * bb)), c1 = asf(__builtin_amdgcn_readlane(asu(sc), 2 * bb + 1));
        const float o0 = fmaf(c0, asf(g[2 * bb][0]), c1 * asf(g[2 * bb + 1][0])), o1 = fmaf(c0, asf(g[2 * bb][2]), c1 * asf(g[2 * bb + 1][2]));
        ldsu[L_OS + bb * 64 + lane] = pkbf(o0, o1);
      }
      lds_set(flg + 26, lane);
      STAMP(10);
    } else {
      const int r = h * 3 + t - 5;
      if (r < 80) {
        const int rb = r / 20, rc = r - 20 * rb;
        u32x4 g[8];
        int off[8];
#pragma unroll
        for (int u = 0; u < 8; ++u) { off[u] = WS_YP + ((rb * 5 + (rc >> 2)) * 32 + u * 4 + (lane >> 4)) * 1024 + (rc & 3) * 256 + (lane & 15) * 16; g[u] = u32x4{0u, 0u, 0u, 0u}; }
        __builtin_amdgcn_s_sleep(RDELAY);
        STAMP(15);
        const u32 sp_ = gather<8, RNAP>(rs, off, g, tag);
        STAMPV(16, sp_); (void)sp_;
        float y0 = 0.f, y1 = 0.f;
#pragma unroll
        for (int u = 0; u < 8; ++u) { y0 += asf(g[u][0]); y1 += asf(g[u][2]); }
        y0 = red16(y0, y0); y1 = red16(y1, y1);
        y0 = red32(y0, y0); y1 = red32(y1, y1);
        if (lane < 16) *reinterpret_cast<u32*>(p.out + (rb * DM + 32 * rc + 2 * lane) * 2) = pkbf(y0, y1);
        STAMP(9);
      } else if (wg == 255) {
        u32x4 g[4];
        int off[4];
#pragma unroll
        for (int u = 0; u < 4; ++u) { off[u] = WS_OX + (u * 64 + lane) * OXS + 1024; g[u] = u32x4{0u, 0u, 0u, 0u}; }
        gather<4, 4>(rs, off, g, tag);
        if (lane == 0) __builtin_amdgcn_raw_buffer_store_b32(tag, rs, WS_E, 0, AUX_ST);
      }
    }
    STAMP_OUT();
    return;
  }

  u32x4 mk;
#pragma unroll
  for (int k = 0; k < 4; ++k) mk[k] = ((lane >> 1) & 3) == k ? ((lane & 1) ? 0xffff0000u : 0xffffu) : 0u;
  {
    u32* xs = ldsu + L_XS + wave * 160;
#pragma unroll
    for (int k = 0; k < 2; ++k) {
      xs[(4 * xc + 2 * k) * 4 + xb] = __builtin_amdgcn_perm(xr[k], xr[k], 0x01000100u);
      xs[(4 * xc + 2 * k + 1) * 4 + xb] = __builtin_amdgcn_perm(xr[k], xr[k], 0x03020302u);
    }
    asm volatile("s_waitcnt lgkmcnt(0)" ::: "memory");
    u32x4 xv[5];
#pragma unroll
    for (int i = 0; i < 5; ++i) xv[i] = *reinterpret_cast<const u32x4*>(xs + ((lane >> 3) + i * 8) * 4);
    f32x4 d[4];
#pragma unroll
    for (int bb = 0; bb < 4; ++bb) d[bb] = f32x4{0.f, 0.f, 0.f, 0.f};
#pragma unroll
    for (int i = 0; i < 5; ++i) {
      if (i == 0) { THR(4 + KPRE); STAMP(21); }
      if (i < KMID) { THR(4 + KPRE); K[KPRE + i] = ldnt(kp + (KPRE + i) * 1024); SB(); }
#pragma unroll
      for (int bb = 0; bb < 4; ++bb) d[bb] = gemv_mfma(d[bb], xv[i][bb], mk, S1[i]);
    }
    STAMP(22);
    f32x4 r;
#pragma unroll
    for (int k = 0; k < 4; ++k) {
      auto s0 = __builtin_amdgcn_permlane32_swap(asu(d[0][k]), asu(d[1][k]), false, false);
      auto s1 = __builtin_amdgcn_permlane32_swap(asu(d[2][k]), asu(d[3][k]), false, false);
      const float t0 = asf(s0[0]) + dpp<XOR8>(asf(s0[1])), t1 = asf(s1[1]) + dpp<XOR8>(asf(s1[0]));
      r[k] = (lane & 8) ? t1 : t0;
    }
    *reinterpret_cast<f32x4*>(lds + L_R1 + wave * 256 + (((lane >> 3) & 1) * 2 + (lane >> 5)) * 64 + (lane & 7) * 8 + ((lane >> 4) & 1) * 4) = r;
    lds_set(flg + wave, lane);
  }
  SB();
  const __amdgpu_buffer_rsrc_t rd = __builtin_amdgcn_make_buffer_rsrc((void*)(p.wd + (size_t)(54 * h) * (DM * 2)), 0, wown ? 54 * DM * 2 : 0, 0x00020000);
  const __amdgpu_buffer_rsrc_t ro = __builtin_amdgcn_make_buffer_rsrc((void*)(p.wo + (size_t)(128 * h) * (DM * 2)), 0, wown ? 128 * DM * 2 : 0, 0x00020000);
  const int wdo = (8 * wave + (lane >> 4)) * (DM * 2) + 256 * t + (lane & 15) * 16;
  const int woo = (16 * wave + (lane >> 4)) * (DM * 2) + 256 * t + (lane & 15) * 16;
#define CL(n_, lo_, hi_) ((n_) >= (lo_) && (n_) < (hi_) ? (n_) - (lo_) : 0)
#define SLOAD(n_) do { \
    if ((n_) < 16) V[CL(n_, 0, 16)] = ldnt(vp + (n_) * 1024); \
    else if ((n_) < 18) W[CL(n_, 16, 22)] = __builtin_amdgcn_raw_buffer_load_b128(rd, wdo + ((n_) - 16) * (DM * 8), 0, 2); \
    else if ((n_) < 22) W[CL(n_, 16, 22)] = __builtin_amdgcn_raw_buffer_load_b128(ro, woo + ((n_) - 18) * (DM * 8), 0, 2); \
  } while (0)
#pragma unroll
  for (int i = KPRE + KMID; i < 16; ++i) { THR(NTH - 1); K[i] = ldnt(kp + i * 1024); }
  STAMP(13);
#pragma unroll
  for (int i = 0; i < VPRE; ++i) { THR(NTH - 1); SLOAD(i); }
  SB();
  lds_wait<1>(flg + 24, 1, lane);
  float pe;
  {
    const u32x4 q = *reinterpret_cast<const u32x4*>(ldsu + L_QS + (lane & 15) * 4);
    float x[16];
#pragma unroll
    for (int i = 0; i < 16; ++i) { float d = 0.f;
#pragma unroll
      for (int k = 0; k < 4; ++k) d = dot2(K[i][k], q[k], d);
      x[i] = d; }
    SB();
    SLOAD(VPRE);
    SB();
#define TR_STEP(HALF, CTRL) _Pragma("unroll") for (int u = 0; u < (HALF); ++u) { \
      const float t1_ = x[u] + dpp<CTRL>(x[u]), t2_ = x[u + (HALF)] + dpp<CTRL>(x[u + (HALF)]); x[u] = (lane & (HALF)) ? t2_ : t1_; }
    TR_STEP(8, XOR8) TR_STEP(4, XOR7) TR_STEP(2, XOR2) TR_STEP(1, XOR1)
#undef TR_STEP
    const float s = x[0] * (0.08838834764831845f * 1.4426950408889634f);
    float m = s;
    m = fmaxf(m, dpp<XOR1>(m)); m = fmaxf(m, dpp<XOR2>(m)); m = fmaxf(m, dpp<XOR7>(m)); m = fmaxf(m, dpp<XOR8>(m)); m = max16(m); m = max32(m);
    pe = __builtin_amdgcn_exp2f(s - m);
    const float l = wave_sum(pe);
    if (lane == 0) *reinterpret_cast<f32x2*>(lds + L_ML + 2 * wave) = f32x2{m, l};
    lds_set(flg + 8 + wave, lane);
  }
  STAMP(4);
  SB();
  float acc[8];
#pragma unroll
  for (int k = 0; k < 8; ++k) acc[k] = 0.f;
  constexpr int WE = 6 - WLATE;
#define SEQ(n_) ((n_) <= VPRE ? (n_) : (n_) <= VPRE + WE ? (n_) + 15 - VPRE : (n_) <= 15 + WE ? (n_) - WE : (n_))
#define MIN22(a_) ((a_) < 22 ? (a_) : 22)
#define ISS(i_) ((i_) > VPRE ? MIN22((i_) + WE + NTV) : VPRE + 1 + ((i_) * (WE + NTV) + VPRE) / (VPRE + 1))
#define PVS(i_) do { _Pragma("unroll") for (int n_ = ISS(i_); n_ < ISS((i_) + 1); ++n_) { THR(NTV - 1); SLOAD(SEQ(n_)); } \
    SB(); pv_step(acc, PV<i_>::bc(pe), V[i_]); PIN8(acc); SB(); } while (0)
  PVS(0); PVS(1); PVS(2); PVS(3); PVS(4); PVS(5); PVS(6); PVS(7);
  PVS(8); PVS(9); PVS(10); PVS(11); PVS(12); PVS(13); PVS(14); PVS(15);
#pragma unroll
  for (int i = ISS(16); i < 22; ++i) SLOAD(SEQ(i));
  SB();
  {
    float u4[4];
#pragma unroll
    for (int k = 0; k < 4; ++k) u4[k] = red32(acc[k], acc[k + 4]);
    *reinterpret_cast<f32x2*>(lds + L_REC + wave * 128 + 8 * (lane & 15) + 2 * (lane >> 4)) = f32x2{red16(u4[0], u4[2]), red16(u4[1], u4[3])};
    lds_set(flg + 16 + wave, lane);
  }
  STAMP(5);
  SB();
  if (!wown) { STAMP_OUT(); return; }
  {
    u32* wr = ldsu + L_W + (lane >> 4) * WROW + (lane & 15) * 4;
#pragma unroll
    for (int n = 0; n < 2; ++n) *reinterpret_cast<u32x4*>(wr + (128 + 8 * wave + 4 * n) * WROW) = W[n];
#pragma unroll
    for (int n = 0; n < 4; ++n) *reinterpret_cast<u32x4*>(wr + (16 * wave + 4 * n) * WROW) = W[2 + n];
    lds_set(flg + 28 + wave, lane);
  }
  lds_wait<1>(flg + 28, 8, lane);
  u32x4 B[6];
  {
    const u32* rd_ = ldsu + L_W + (8 * (lane >> 4) + ((lane >> 2) & 3)) * WROW + 8 * wave + 2 * (lane & 3);
#pragma unroll
    for (int s = 0; s < 6; ++s) { const u32x2 lo = lds_tr(rd_ + 32 * s * WROW), hi = lds_tr(rd_ + (32 * s + 4) * WROW); B[s] = u32x4{lo[0], lo[1], hi[0], hi[1]}; }
  }
  lds_wait<1>(flg + 25, 1, lane);
  const u32 tag = ldsu[L_MISC + 4];
  const int g = lane >> 4;
  const float gate = lds[L_MISC + g];
  const f32x4 z4 = {0.f, 0.f, 0.f, 0.f};
  float ym;
  {
    const u32* hs = ldsu + L_HS + (lane & 3) * 32 + 4 * g;
    const f32x4 d = mfma(*reinterpret_cast<const u32x4*>(hs + 16), B[5], mfma(*reinterpret_cast<const u32x4*>(hs), B[4], z4));
    ym = (1.f - gate) * (g == 0 ? d[0] : g == 1 ? d[1] : g == 2 ? d[2] : d[3]);
  }
  asm volatile("" : "+v"(ym), "+v"(B[0]), "+v"(B[1]), "+v"(B[2]), "+v"(B[3]));
  STAMP(3);
  SB();
  lds_wait<0>(flg + 26, 1, lane);
  {
    const u32* os = ldsu + L_OS + (lane & 3) * 64 + 4 * g;
    u32x4 a[4];
#pragma unroll
    for (int s = 0; s < 4; ++s) a[s] = *reinterpret_cast<const u32x4*>(os + 16 * s);
    const f32x4 d = mfma(a[2], B[2], mfma(a[0], B[0], z4)) + mfma(a[3], B[3], mfma(a[1], B[1], z4));
    const float y = fmaf(gate, g == 0 ? d[0] : g == 1 ? d[1] : g == 2 ? d[2] : d[3], ym);
    XST2(u32x2{asu(y), tag}, rs, WS_YP + ((g * 5 + t) * 32 + h) * 1024 + wave * 128 + (lane & 15) * 8, 0, AUX_ST);
  }
  STAMP(6);
  STAMP_OUT();
}

extern "C" void kernel_launch(void* const* d_in, const int* in_sizes, int n_in, void* d_out, int out_size, void* d_ws, size_t ws_size,
                              hipStream_t stream) {
  Params p;
  p.x = (const char*)d_in[0]; p.k = (const char*)d_in[1]; p.v = (const char*)d_in[2]; p.wq = (const char*)d_in[3]; p.wo = (const char*)d_in[4];
  p.wg = (const char*)d_in[6]; p.wu = (const char*)d_in[7]; p.wd = (const char*)d_in[8]; p.gw = (const char*)d_in[9];
  p.out = (char*)d_out; p.ws = (char*)d_ws;
  hipLaunchKernelGGL(fused_decode, dim3(256), dim3(NTHR), 0, stream, p);
}
```

```cpp
#include <hip/hip_runtime.h>
#include <hip/hip_bf16.h>
#include <stdint.h>

typedef unsigned u32;
using u32x4 = __attribute__((ext_vector_type(4))) unsigned;
using u32x2 = __attribute__((ext_vector_type(2))) unsigned;
using f32x4 = __attribute__((ext_vector_type(4))) float;
using f32x2 = __attribute__((ext_vector_type(2))) float;
using bf16x2 = __attribute__((ext_vector_type(2))) __bf16;
using bf16x8 = __attribute__((ext_vector_type(8))) __bf16;
using s16x4 = __attribute__((ext_vector_type(4))) short;

constexpr int DM = 640, NH = 32, SK = 4096, WIN = 1024, DF = 1728;
constexpr int NTHR = 576;
#define AUX_LD 16
#define AUX_ST 16
#define KPRE 0
#define KMID 4
#define VPRE 5
#define NTH 4
#define NTV NHI
#define WLATE 2
#define QMID 3
#define RDELAY 16
#define MAPV 0
#define NHI 6
#define NLO 1
#define TB 200
#define WPOST 0
#define OFLAG 17
#define ODELAY 8
#define PO 34
#define PN 35
#define QDELAY 16
constexpr u32 SPIN_CAP = 1u << 18;

constexpr int WS_E = 0;
constexpr int WS_QP = 4096;
constexpr int WS_GP = WS_QP + 32 * 4 * 2 * 128 * 8;
constexpr int OXS = 1152;
constexpr int WS_OX = WS_GP + 2 * 2 * 4 * DF * 8;
constexpr int WS_YP = WS_OX + 256 * OXS;
constexpr int WS_END = WS_YP + 4 * 5 * 32 * 128 * 8;
constexpr int OOB = 0x7ffffff0;

constexpr int L_XS = 0;
constexpr int L_R1 = L_XS + 8 * 160;
constexpr int L_QS = L_R1 + 2048;
constexpr int L_HS = L_QS + 64;
constexpr int L_OS = L_HS + 128;
constexpr int L_MISC = L_OS + 256;
constexpr int L_FLG = L_MISC + 8;
constexpr int L_ML = L_FLG + 36;
constexpr int L_FW = L_ML + 16;
constexpr int L_OWN = L_FW + 8;
constexpr int L_REC = L_OWN + 128;
constexpr int WROW = 72;
constexpr int L_W = L_REC + 1024;
constexpr int L_END = L_W + 192 * WROW;

struct Params {
  const char* x; const char* k; const char* v; const char* wq; const char* wo;
  const char* wg; const char* wu; const char* wd; const char* gw; char* out; char* ws;
};

#define STAMP(i)
#define STAMPV(i, v)
#define STAMP_DECL
#define STAMP_OUT()
#define SB() __builtin_amdgcn_sched_barrier(0)
#define PIN8(a) asm volatile("" : "+v"(a[0]), "+v"(a[1]), "+v"(a[2]), "+v"(a[3]), "+v"(a[4]), "+v"(a[5]), "+v"(a[6]), "+v"(a[7]))
#define THR(n) do { SB(); __builtin_amdgcn_s_waitcnt(((n) & 15) | (7 << 4) | (15 << 8) | (((n) >> 4) << 14)); SB(); } while (0)
#define THRA() do { SB(); unsigned long long n_; \
    asm volatile("s_memrealtime %0\n\ts_waitcnt vmcnt(%1) lgkmcnt(0)" : "=s"(n_) : "n"(NHI - 1)); \
    const int d_ = (int)((u32)n_ - due); due += pace; \
    asm volatile("s_cmp_gt_i32 %0, 0\n\ts_cbranch_scc1 1f\n\ts_waitcnt vmcnt(%1)\n1:" :: "s"(d_), "n"(NLO - 1) : "scc"); SB(); } while (0)
#define CLOB() asm volatile("" ::: "memory")
#define WG_BARRIER() do { asm volatile("s_waitcnt lgkmcnt(0)" ::: "memory"); __builtin_amdgcn_s_barrier(); CLOB(); } while (0)
#define XST __builtin_amdgcn_raw_buffer_store_b128
#define XST2 __builtin_amdgcn_raw_buffer_store_b64
#define XLD4 __builtin_amdgcn_raw_buffer_load_b128

__device__ __forceinline__ u32x4 ldnt(const char* p) { return __builtin_nontemporal_load(reinterpret_cast<const u32x4*>(p)); }
__device__ __forceinline__ float asf(u32 x) { return __builtin_bit_cast(float, x); }
__device__ __forceinline__ u32 asu(float x) { return __builtin_bit_cast(u32, x); }
__device__ __forceinline__ float lo16(u32 x) { return asf(x << 16); }
__device__ __forceinline__ float hi16(u32 x) { return asf(x & 0xffff0000u); }
__device__ __forceinline__ float dot2(u32 a, u32 b, float acc) {
  return __builtin_amdgcn_fdot2_f32_bf16(__builtin_bit_cast(bf16x2, a), __builtin_bit_cast(bf16x2, b), acc, false);
}
__device__ __forceinline__ u32 pkbf(float lo, float hi) {
  bf16x2 r; r[0] = (__bf16)lo; r[1] = (__bf16)hi; return __builtin_bit_cast(u32, r);
}
__device__ __forceinline__ float rbf(float x) { return lo16(pkbf(x, 0.f)); }
template <int CTRL> __device__ __forceinline__ float dpp(float x) {
  return asf(__builtin_amdgcn_mov_dpp(__builtin_bit_cast(int, x), CTRL, 0xf, 0xf, true));
}
constexpr int XOR1 = 0xB1, XOR2 = 0x4E, XOR7 = 0x141, XOR8 = 0x128, BCAST = 0x150;
__device__ __forceinline__ float red32(float a, float b) {
  auto s = __builtin_amdgcn_permlane32_swap(asu(a), asu(b), false, false); return asf(s[0]) + asf(s[1]);
}
__device__ __forceinline__ float red16(float a, float b) {
  auto s = __builtin_amdgcn_permlane16_swap(asu(a), asu(b), false, false); return asf(s[0]) + asf(s[1]);
}
__device__ __forceinline__ float max32(float a) {
  auto s = __builtin_amdgcn_permlane32_swap(asu(a), asu(a), false, false); return fmaxf(asf(s[0]), asf(s[1]));
}
__device__ __forceinline__ float max16(float a) {
  auto s = __builtin_amdgcn_permlane16_swap(asu(a), asu(a), false, false); return fmaxf(asf(s[0]), asf(s[1]));
}
__device__ __forceinline__ float wave_sum(float s) {
  s += dpp<XOR1>(s); s += dpp<XOR2>(s); s += dpp<XOR7>(s); s += dpp<XOR8>(s); s = red16(s, s); return red32(s, s);
}
__device__ __forceinline__ float geglu(float g, float u) {
  const float ge = rbf(0.5f * g * (1.f + tanhf(0.7978845608028654f * (g + 0.044715f * g * g * g))));
  return ge * u;
}

__device__ __forceinline__ void pv_step(float (&acc)[8], float p, const u32x4& v) {
#pragma unroll
  for (int k = 0; k < 4; ++k) { acc[2 * k] = fmaf(p, lo16(v[k]), acc[2 * k]); acc[2 * k + 1] = fmaf(p, hi16(v[k]), acc[2 * k + 1]); }
}
__device__ __forceinline__ f32x4 gemv_mfma(f32x4 d, u32 mv, const u32x4& mk, const u32x4& w) {
  const u32x4 a = {mv & mk[0], mv & mk[1], mv & mk[2], mv & mk[3]};
  return __builtin_amdgcn_mfma_f32_16x16x32_bf16(__builtin_bit_cast(bf16x8, a), __builtin_bit_cast(bf16x8, w), d, 0, 0, 0);
}
__device__ __forceinline__ f32x4 mfma(const u32x4& a, const u32x4& b, f32x4 d) {
  return __builtin_amdgcn_mfma_f32_16x16x32_bf16(__builtin_bit_cast(bf16x8, a), __builtin_bit_cast(bf16x8, b), d, 0, 0, 0);
}
__device__ __forceinline__ u32x2 lds_tr(const u32* p) {
  return __builtin_bit_cast(u32x2, __builtin_amdgcn_ds_read_tr16_b64_v4i16((__attribute__((address_space(3))) s16x4*)p));
}
template <int N, int NAP>
__device__ __forceinline__ u32 gather(const __amdgpu_buffer_rsrc_t rs, int (&off)[N], u32x4 (&g)[N], u32 tag) {
  u32 spins = 0;
#pragma unroll 1
  for (;; ++spins) {
    CLOB();
    u32x4 n[N];
#pragma unroll
    for (int u = 0; u < N; ++u) n[u] = XLD4(rs, off[u], 0, AUX_LD);
    bool ok = true;
#pragma unroll
    for (int u = 0; u < N; ++u) {
      const bool live = off[u] != OOB, hit = (n[u][1] == tag) & (n[u][3] == tag);
      g[u][0] = live ? n[u][0] : g[u][0]; g[u][2] = live ? n[u][2] : g[u][2];
      off[u] = hit ? OOB : off[u];
      ok &= hit | !live;
    }
    if (__all(ok) || spins > SPIN_CAP) break;
    __builtin_amdgcn_s_sleep(NAP);
  }
  return spins;
}
template <int I> struct PV {
  static __device__ __forceinline__ float bc(float p) { return dpp<BCAST + I>(p); }
};

typedef volatile __attribute__((address_space(3))) u32 vlds_u32;
__device__ __forceinline__ void lds_set(u32* f, int lane) { CLOB(); if (lane == 0) *(vlds_u32*)f = 1u; CLOB(); }
template <int NAP>
__device__ __forceinline__ void lds_wait(u32* f, int n, int lane) {
  vlds_u32* q = (vlds_u32*)(f + (lane < n ? lane : 0));
#pragma unroll 1
  for (u32 spins = 0; spins < (SPIN_CAP << 4); ++spins) { if (__all(*q != 0u)) break; if (NAP) __builtin_amdgcn_s_sleep(NAP); }
  CLOB();
}

__global__ void __launch_bounds__(NTHR, 3) fused_decode(Params p) {
  __shared__ __attribute__((aligned(16))) float lds[L_END];
  const int bid = blockIdx.x, tid = threadIdx.x, lane = tid & 63;
  const int wave = __builtin_amdgcn_readfirstlane(tid >> 6);
  const int xcd = bid & 7, slot = bid >> 3;
  const int h = xcd * 4 + (slot >> 3), t = slot & 7;
  const int b = t >> 1, j = t & 1, wg = h * 8 + t;
  const bool isq = t >= 4;
  const int mt = t * 32 + h;
  const bool s1on = isq || mt < 108;
  const int mm = mt >= 54 ? 1 : 0, mc = (mt - 54 * mm) >> 1, rh = isq ? ((t >> 1) & 1) : (mt & 1);
  const bool wown = t < 5;
  const __amdgpu_buffer_rsrc_t rs = __builtin_amdgcn_make_buffer_rsrc((void*)p.ws, 0, WS_END, 0x00020000);
  u32* ldsu = reinterpret_cast<u32*>(lds);
  u32* flg = ldsu + L_FLG;
  STAMP_DECL;
  STAMP(0);
  u32 due = (u32)__builtin_amdgcn_s_memrealtime() + TB;
  const u32 pace = wown ? PO : PN;

  const int row0 = 320 * rh + 40 * (wave & 7);
  u32x4 S1[5], K[16], V[16], W[6];
  u32x2 xr = {0u, 0u};
  u32 ev = 0u;
  const int xb = min(lane / 10, 3), xc = min(lane - 10 * xb, 9);
  const size_t kvoff = ((size_t)(b * NH + h) * SK + (SK - WIN) + j * 512 + (wave & 7) * 64) * 256 + lane * 16;
  const char* kp = p.k + kvoff;
  const char* vp = p.v + kvoff;
  if (wave < 8) {
    xr = *reinterpret_cast<const u32x2*>(p.x + xb * (DM * 2) + (row0 + 4 * xc) * 2);
    const char* sp; int sstep;
    if (isq) { sp = p.wq + (size_t)(row0 + (lane >> 3)) * 8192 + h * 256 + (t & 1) * 128 + (lane & 7) * 16; sstep = 8 * 8192; }
    else { sp = (mm ? p.wu : p.wg) + (size_t)(row0 + (lane >> 3)) * (DF * 2) + mc * 128 + (lane & 7) * 16; sstep = 8 * DF * 2; }
    if (!s1on) { sp = p.x + (lane & 15) * 16; sstep = 0; }
#pragma unroll
    for (int i = 0; i < 5; ++i) S1[i] = ldnt(sp + i * sstep);
    SB();
#pragma unroll
    for (int i = 0; i < KPRE; ++i) K[i] = ldnt(kp + i * 1024);
    STAMP(18);
  } else {
    ev = __builtin_amdgcn_raw_buffer_load_b32(rs, WS_E, 0, 0);
    if (lane < 36) flg[lane] = 0u;
  }
  SB();
  WG_BARRIER();
  SB();
  STAMP(20);

  if (__builtin_expect(wave == 8, 0)) {
    __builtin_amdgcn_s_setprio(3);
    const u32 tag = __builtin_amdgcn_readfirstlane(ev) + 1u;
    lds_wait<1>(flg + 0, 8, lane);
    STAMP(11);
    if (s1on) {
#pragma unroll
      for (int u = 0; u < 2; ++u) {
        const float* d = lds + L_R1 + u * 128 + lane * 2;
        f32x2 s = *reinterpret_cast<const f32x2*>(d);
#pragma unroll
        for (int w = 1; w < 8; ++w) s += *reinterpret_cast<const f32x2*>(d + w * 256);
        const int bb = 2 * u + (lane >> 5);
        const int off = isq ? WS_QP + ((h * 4 + bb) * 2 + rh) * 1024 + (t & 1) * 512 : WS_GP + (((mm * 2 + rh) * 4 + bb) * DF + mc * 64) * 8;
        XST(u32x4{asu(s[0]), tag, asu(s[1]), tag}, rs, off + (lane & 31) * 16, 0, AUX_ST);
      }
    }
    STAMP(1);
    __builtin_amdgcn_s_sleep(QDELAY);
    {
      u32x4 g[2];
      int off[2];
#pragma unroll
      for (int u = 0; u < 2; ++u) { off[u] = WS_QP + ((h * 4 + b) * 2 + u) * 1024 + lane * 16; g[u] = u32x4{0u, 0u, 0u, 0u}; }
      const u32 sp_ = gather<2, 2>(rs, off, g, tag);
      STAMPV(12, sp_); (void)sp_;
      ldsu[L_QS + lane] = pkbf(asf(g[0][0]) + asf(g[1][0]), asf(g[0][2]) + asf(g[1][2]));
      if (lane == 0) ldsu[L_MISC + 4] = tag;
      lds_set(flg + 24, lane);
      STAMP(7);
    }
    if (wown) {
      const int i = lane & 31, m = lane >> 5;
      {
        float s = 0.f;
#pragma unroll
        for (int c = 0; c < 5; ++c) {
          const u32x4 xv = *reinterpret_cast<const u32x4*>(p.x + (lane >> 4) * (DM * 2) + (c * 16 + (lane & 15)) * 16);
          const u32x4 gv = *reinterpret_cast<const u32x4*>(p.gw + (c * 16 + (lane & 15)) * 16);
#pragma unroll
          for (int k = 0; k < 4; ++k) s = dot2(xv[k], gv[k], s);
        }
        s += dpp<XOR1>(s); s += dpp<XOR2>(s); s += dpp<XOR7>(s); s += dpp<XOR8>(s);
        if ((lane & 15) == 0) lds[L_MISC + (lane >> 4)] = 1.f / (1.f + __expf(-s));
      }
      u32x4 g[8];
      int off[8];
#pragma unroll
      for (int u = 0; u < 8; ++u) { off[u] = i < 27 ? WS_GP + ((m * 8 + u) * DF + 54 * h + 2 * i) * 8 : OOB; g[u] = u32x4{0u, 0u, 0u, 0u}; }
      gather<8, 8>(rs, off, g, tag);
#pragma unroll
      for (int bb = 0; bb < 4; ++bb) {
        const float a0 = rbf(asf(g[bb][0]) + asf(g[4 + bb][0])), a1 = rbf(asf(g[bb][2]) + asf(g[4 + bb][2]));
        auto s = __builtin_amdgcn_permlane32_swap(asu(a0), asu(a1), false, false);
        const float hv = geglu(asf(s[0]), asf(s[1]));
        reinterpret_cast<unsigned short*>(ldsu + L_HS)[bb * 64 + 2 * i + m] = (unsigned short)pkbf(hv, 0.f);
      }
      lds_set(flg + 25, lane);
    }
    STAMP(8);
    lds_wait<1>(flg + 8, 8, lane);
    {
      const f32x2 ml = *reinterpret_cast<const f32x2*>(lds + L_ML + 2 * (lane & 7));
      float M = ml[0];
      M = fmaxf(M, dpp<XOR1>(M)); M = fmaxf(M, dpp<XOR2>(M)); M = fmaxf(M, dpp<XOR7>(M));
      const float fw = __builtin_amdgcn_exp2f(ml[0] - M);
      float L = fw * ml[1];
      L += dpp<XOR1>(L); L += dpp<XOR2>(L); L += dpp<XOR7>(L);
      if (lane < 8) lds[L_FW + lane] = fw;
      lds_set(flg + 18, lane);
      if (lane == 0) XST(u32x4{asu(M), tag, asu(L), tag}, rs, WS_OX + wg * OXS + 1024, 0, AUX_ST);
    }
    STAMP(2);
    if (wown) {
      float c[8];
      {
        u32x4 g[1] = {u32x4{0u, 0u, 0u, 0u}};
        int off[1] = {lane < 8 ? WS_OX + (h * 8 + lane) * OXS + 1024 : OOB};
        __builtin_amdgcn_s_sleep(16);
        gather<1, 8>(rs, off, g, tag);
        const float mu = asf(g[0][0]);
        float sc = __builtin_amdgcn_exp2f(mu - fmaxf(mu, dpp<XOR1>(mu)));
        const float sl = sc * asf(g[0][2]);
        sc = sc / (sl + dpp<XOR1>(sl));
#pragma unroll
        for (int u = 0; u < 8; ++u) c[u] = asf(__builtin_amdgcn_readlane(asu(sc), u));
      }
      STAMP(19);
      lds_wait<1>(flg + OFLAG, 1, lane);
      if (ODELAY) __builtin_amdgcn_s_sleep(ODELAY);
      u32x4 g[8];
#pragma unroll
      for (int u = 0; u < 8; ++u) g[u] = u32x4{0u, 0u, 0u, 0u};
      const int off = WS_OX + h * 8 * OXS + lane * 16;
      u32 miss = 0xffu, todo = 0xfu, spins = 0;
#pragma unroll 1
      for (;; ++spins) {
        CLOB();
        const u32 far = miss & ~(1u << t);
#pragma unroll
        for (int u = 0; u < 8; ++u) if ((far >> u) & 1) g[u] = XLD4(rs, off + u * OXS, 0, AUX_LD);
#pragma unroll
        for (int u = 0; u < 8; ++u) if ((far >> u) & 1) { if (__all((g[u][1] == tag) & (g[u][3] == tag))) miss &= ~(1u << u); }
        if ((miss >> t) & 1) {
          const u32 fl = *(vlds_u32*)(flg + 17);
          CLOB();
          const u32x2 own = *reinterpret_cast<const u32x2*>(ldsu + L_OWN + 2 * lane);
          if (__builtin_amdgcn_readfirstlane(fl)) {
            miss &= ~(1u << t);
#pragma unroll
            for (int u = 0; u < 5; ++u) if (u == t) { g[u][0] = own[0]; g[u][2] = own[1]; }
          }
        }
        if (spins > SPIN_CAP) miss = 0u;
#pragma unroll
        for (int bb = 0; bb < 4; ++bb) if (((todo >> bb) & 1) && !((miss >> (2 * bb)) & 3)) {
          const float o0 = fmaf(c[2 * bb], asf(g[2 * bb][0]), c[2 * bb + 1] * asf(g[2 * bb + 1][0]));
          const float o1 = fmaf(c[2 * bb], asf(g[2 * bb][2]), c[2 * bb + 1] * asf(g[2 * bb + 1][2]));
          ldsu[L_OS + bb * 64 + lane] = pkbf(o0, o1);
          todo &= ~(1u << bb);
        }
        if (!miss) break;
      }
      lds_set(flg + 26, lane);
      STAMP(10);
      STAMPV(17, spins);
    } else {
      const int r = h * 3 + t - 5;
      if (r < 80) {
        const int rb = r / 20, rc = r - 20 * rb;
        u32x4 g[8];
#pragma unroll
        for (int u = 0; u < 8; ++u) g[u] = u32x4{0u, 0u, 0u, 0u};
        const int off = WS_YP + ((rb * 5 + (rc >> 2)) * 32 + (lane >> 4)) * 1024 + (rc & 3) * 256 + (lane & 15) * 16;
        lds_wait<1>(flg + 17, 1, lane);
        __builtin_amdgcn_s_sleep(RDELAY);
        STAMP(15);
        u32 miss = 0xffu, spins = 0;
#pragma unroll 1
        for (;; ++spins) {
          CLOB();
          const u32 far = miss;
#pragma unroll
          for (int u = 0; u < 8; ++u) if ((far >> u) & 1) g[u] = XLD4(rs, off + u * 4096, 0, AUX_LD);
#pragma unroll
          for (int u = 0; u < 8; ++u) if ((far >> u) & 1) { if (__all((g[u][1] == tag) & (g[u][3] == tag))) miss &= ~(1u << u); }
          if (!miss || spins > SPIN_CAP) break;
        }
        STAMPV(16, spins);
        float y0 = ((asf(g[0][0]) + asf(g[1][0])) + (asf(g[2][0]) + asf(g[3][0]))) + ((asf(g[4][0]) + asf(g[5][0])) + (asf(g[6][0]) + asf(g[7][0])));
        float y1 = ((asf(g[0][2]) + asf(g[1][2])) + (asf(g[2][2]) + asf(g[3][2]))) + ((asf(g[4][2]) + asf(g[5][2])) + (asf(g[6][2]) + asf(g[7][2])));
        y0 = red16(y0, y0); y1 = red16(y1, y1);
        y0 = red32(y0, y0); y1 = red32(y1, y1);
        if (lane < 16) *reinterpret_cast<u32*>(p.out + (rb * DM + 32 * rc + 2 * lane) * 2) = pkbf(y0, y1);
        STAMP(9);
      } else if (wg == 255) {
        u32x4 g[4];
        int off[4];
#pragma unroll
        for (int u = 0; u < 4; ++u) { off[u] = WS_OX + (u * 64 + lane) * OXS + 1024; g[u] = u32x4{0u, 0u, 0u, 0u}; }
        gather<4, 4>(rs, off, g, tag);
        if (lane == 0) __builtin_amdgcn_raw_buffer_store_b32(tag, rs, WS_E, 0, AUX_ST);
      }
    }
    STAMP_OUT();
    return;
  }

  u32x4 mk;
#pragma unroll
  for (int k = 0; k < 4; ++k) mk[k] = ((lane >> 1) & 3) == k ? ((lane & 1) ? 0xffff0000u : 0xffffu) : 0u;
  {
    u32* xs = ldsu + L_XS + wave * 160;
#pragma unroll
    for (int k = 0; k < 2; ++k) {
      xs[(4 * xc + 2 * k) * 4 + xb] = __builtin_amdgcn_perm(xr[k], xr[k], 0x01000100u);
      xs[(4 * xc + 2 * k + 1) * 4 + xb] = __builtin_amdgcn_perm(xr[k], xr[k], 0x03020302u);
    }
    asm volatile("s_waitcnt lgkmcnt(0)" ::: "memory");
    u32x4 xv[5];
#pragma unroll
    for (int i = 0; i < 5; ++i) xv[i] = *reinterpret_cast<const u32x4*>(xs + ((lane >> 3) + i * 8) * 4);
    f32x4 d[4];
#pragma unroll
    for (int bb = 0; bb < 4; ++bb) d[bb] = f32x4{0.f, 0.f, 0.f, 0.f};
#pragma unroll
    for (int i = 0; i < 5; ++i) {
      if (i == 0) { THR(4 + KPRE); STAMP(21); }
      if (i < KMID) { THR(4 + KPRE); K[KPRE + i] = ldnt(kp + (KPRE + i) * 1024); SB(); }
#pragma unroll
      for (int bb = 0; bb < 4; ++bb) d[bb] = gemv_mfma(d[bb], xv[i][bb], mk, S1[i]);
    }
    STAMP(22);
    f32x4 r;
#pragma unroll
    for (int k = 0; k < 4; ++k) {
      auto s0 = __builtin_amdgcn_permlane32_swap(asu(d[0][k]), asu(d[1][k]), false, false);
      auto s1 = __builtin_amdgcn_permlane32_swap(asu(d[2][k]), asu(d[3][k]), false, false);
      const float t0 = asf(s0[0]) + dpp<XOR8>(asf(s0[1])), t1 = asf(s1[1]) + dpp<XOR8>(asf(s1[0]));
      r[k] = (lane & 8) ? t1 : t0;
    }
    *reinterpret_cast<f32x4*>(lds + L_R1 + wave * 256 + (((lane >> 3) & 1) * 2 + (lane >> 5)) * 64 + (lane & 7) * 8 + ((lane >> 4) & 1) * 4) = r;
    lds_set(flg + wave, lane);
  }
  SB();
  const __amdgpu_buffer_rsrc_t rd = __builtin_amdgcn_make_buffer_rsrc((void*)(p.wd + (size_t)(54 * h) * (DM * 2)), 0, wown ? 54 * DM * 2 : 0, 0x00020000);
  const __amdgpu_buffer_rsrc_t ro = __builtin_amdgcn_make_buffer_rsrc((void*)(p.wo + (size_t)(128 * h) * (DM * 2)), 0, wown ? 128 * DM * 2 : 0, 0x00020000);
  const int wdo = (8 * wave + (lane >> 4)) * (DM * 2) + 256 * t + (lane & 15) * 16;
  const int woo = (16 * wave + (lane >> 4)) * (DM * 2) + 256 * t + (lane & 15) * 16;
#define CL(n_, lo_, hi_) ((n_) >= (lo_) && (n_) < (hi_) ? (n_) - (lo_) : 0)
#define SLOAD(n_) do { \
    if ((n_) < 16) V[CL(n_, 0, 16)] = ldnt(vp + (n_) * 1024); \
    else if ((n_) < 18) W[CL(n_, 16, 22)] = __builtin_amdgcn_raw_buffer_load_b128(rd, wdo + ((n_) - 16) * (DM * 8), 0, 2); \
    else if ((n_) < 22) W[CL(n_, 16, 22)] = __builtin_amdgcn_raw_buffer_load_b128(ro, woo + ((n_) - 18) * (DM * 8), 0, 2); \
  } while (0)
#pragma unroll
  for (int i = KPRE + KMID; i < 16; ++i) { THRA(); K[i] = ldnt(kp + i * 1024); }
  STAMP(13);
#pragma unroll
  for (int i = 0; i < VPRE; ++i) { THRA(); SLOAD(i); }
  SB();
  lds_wait<1>(flg + 24, 1, lane);
  float pe;
  {
    const u32x4 q = *reinterpret_cast<const u32x4*>(ldsu + L_QS + (lane & 15) * 4);
    float x[16];
#pragma unroll
    for (int i = 0; i < 16; ++i) { float d = 0.f;
#pragma unroll
      for (int k = 0; k < 4; ++k) d = dot2(K[i][k], q[k], d);
      x[i] = d;
      if ((i + 1) * QMID / 16 > i * QMID / 16) { asm volatile("" : "+v"(x[i])); THRA(); SLOAD(VPRE + i * QMID / 16); SB(); }
    }
#define TR_STEP(HALF, CTRL) _Pragma("unroll") for (int u = 0; u < (HALF); ++u) { \
      const float t1_ = x[u] + dpp<CTRL>(x[u]), t2_ = x[u + (HALF)] + dpp<CTRL>(x[u + (HALF)]); x[u] = (lane & (HALF)) ? t2_ : t1_; }
    TR_STEP(8, XOR8) TR_STEP(4, XOR7) TR_STEP(2, XOR2) TR_STEP(1, XOR1)
#undef TR_STEP
    const float s = x[0] * (0.08838834764831845f * 1.4426950408889634f);
    float m = s;
    m = fmaxf(m, dpp<XOR1>(m)); m = fmaxf(m, dpp<XOR2>(m)); m = fmaxf(m, dpp<XOR7>(m)); m = fmaxf(m, dpp<XOR8>(m)); m = max16(m); m = max32(m);
    pe = __builtin_amdgcn_exp2f(s - m);
    const float l = wave_sum(pe);
    if (lane == 0) *reinterpret_cast<f32x2*>(lds + L_ML + 2 * wave) = f32x2{m, l};
    lds_set(flg + 8 + wave, lane);
  }
  STAMP(4);
  SB();
  float acc[8];
#pragma unroll
  for (int k = 0; k < 8; ++k) acc[k] = 0.f;
  constexpr int WE = 6 - WLATE, VL = VPRE + QMID - 1;
#define SEQ(n_) ((n_) <= VL ? (n_) : (n_) <= VL + WE ? (n_) + 15 - VL : (n_) <= 15 + WE ? (n_) - WE : (n_))
#define MIN22(a_) ((a_) < 22 - WPOST ? (a_) : 22 - WPOST)
#define ISS(i_) ((i_) > VL ? MIN22((i_) + WE + NTV) : VL + 1 + ((i_) * (WE + NTV) + VL) / (VL + 1))
#define PVS(i_) do { _Pragma("unroll") for (int n_ = ISS(i_); n_ < ISS((i_) + 1); ++n_) { if (SEQ(n_) < 16) THRA(); else THR(NHI - 1); SLOAD(SEQ(n_)); } \
    SB(); pv_step(acc, PV<i_>::bc(pe), V[i_]); PIN8(acc); SB(); } while (0)
  PVS(0); PVS(1); PVS(2); PVS(3); PVS(4); PVS(5); PVS(6); PVS(7);
  PVS(8); PVS(9); PVS(10); PVS(11); PVS(12); PVS(13);
  lds_wait<1>(flg + 18, 1, lane);
  const float fw = lds[L_FW + wave];
  const u32 tag = ldsu[L_MISC + 4];
  PVS(14); PVS(15);
#pragma unroll
  for (int i = ISS(16); i < 22 - WPOST; ++i) SLOAD(SEQ(i));
  SB();
  {
    float u4[4];
#pragma unroll
    for (int k = 0; k < 4; ++k) u4[k] = red32(acc[k], acc[k + 4]);
    *reinterpret_cast<f32x2*>(lds + L_REC + wave * 128 + 8 * (lane & 15) + 2 * (lane >> 4)) = f32x2{fw * red16(u4[0], u4[2]), fw * red16(u4[1], u4[3])};
    CLOB();
    u32 cnt = 0u;
    if (lane == 0) cnt = __hip_atomic_fetch_add(flg + 16, 1u, __ATOMIC_RELAXED, __HIP_MEMORY_SCOPE_WORKGROUP);
    CLOB();
    f32x2 r[8];
#pragma unroll
    for (int w = 0; w < 8; ++w) r[w] = *reinterpret_cast<const f32x2*>(lds + L_REC + w * 128 + lane * 2);
    asm volatile("" : "+v"(r[0]), "+v"(r[1]), "+v"(r[2]), "+v"(r[3]), "+v"(r[4]), "+v"(r[5]), "+v"(r[6]), "+v"(r[7]));
    if (__builtin_amdgcn_readfirstlane(cnt) == 7u) {
      const f32x2 o = ((r[0] + r[1]) + (r[2] + r[3])) + ((r[4] + r[5]) + (r[6] + r[7]));
      XST(u32x4{asu(o[0]), tag, asu(o[1]), tag}, rs, WS_OX + wg * OXS + lane * 16, 0, AUX_ST);
      *reinterpret_cast<f32x2*>(lds + L_OWN + 2 * lane) = o;
      lds_set(flg + 17, lane);
      STAMP(14);
    }
  }
  STAMP(5);
  SB();
  if (!wown) { STAMP_OUT(); return; }
#pragma unroll
  for (int i = 22 - WPOST; i < 22; ++i) SLOAD(SEQ(i));
  SB();
  {
    u32* wr = ldsu + L_W + (lane >> 4) * WROW + (lane & 15) * 4;
#pragma unroll
    for (int n = 0; n < 2; ++n) *reinterpret_cast<u32x4*>(wr + (128 + 8 * wave + 4 * n) * WROW) = W[n];
#pragma unroll
    for (int n = 0; n < 4; ++n) *reinterpret_cast<u32x4*>(wr + (16 * wave + 4 * n) * WROW) = W[2 + n];
    lds_set(flg + 28 + wave, lane);
  }
  lds_wait<1>(flg + 28, 8, lane);
  u32x4 B[6];
  {
    const u32* rd_ = ldsu + L_W + (8 * (lane >> 4) + ((lane >> 2) & 3)) * WROW + 8 * wave + 2 * (lane & 3);
#pragma unroll
    for (int s = 0; s < 6; ++s) { const u32x2 lo = lds_tr(rd_ + 32 * s * WROW), hi = lds_tr(rd_ + (32 * s + 4) * WROW); B[s] = u32x4{lo[0], lo[1], hi[0], hi[1]}; }
  }
  lds_wait<1>(flg + 25, 1, lane);
  const int g = lane >> 4;
  const float gate = lds[L_MISC + g];
  const f32x4 z4 = {0.f, 0.f, 0.f, 0.f};
  float ym;
  {
    const u32* hs = ldsu + L_HS + ((lane >> 2) & 3) * 32 + 4 * g;
    ym = (1.f - gate) * mfma(*reinterpret_cast<const u32x4*>(hs + 16), B[5], mfma(*reinterpret_cast<const u32x4*>(hs), B[4], z4))[0];
  }
  int yoff = WS_YP + ((g * 5 + t) * 32 + h) * 1024 + wave * 128 + (lane & 15) * 8;
  asm volatile("" : "+v"(ym), "+v"(yoff), "+v"(B[0]), "+v"(B[1]), "+v"(B[2]), "+v"(B[3]));
  STAMP(3);
  SB();
  {
    const u32* os = ldsu + L_OS + ((lane >> 2) & 3) * 64 + 4 * g;
    u32x4 a[4];
#pragma unroll 1
    for (u32 spins = 0; spins < (SPIN_CAP << 4); ++spins) {
      const u32 fl = *(vlds_u32*)(flg + 26);
      CLOB();
#pragma unroll
      for (int s = 0; s < 4; ++s) a[s] = *reinterpret_cast<const u32x4*>(os + 16 * s);
      asm volatile("" : "+v"(a[0]), "+v"(a[1]), "+v"(a[2]), "+v"(a[3]) :: "memory");
      if (__builtin_amdgcn_readfirstlane(fl)) break;
    }
    const float y = fmaf(gate, mfma(a[2], B[2], mfma(a[0], B[0], z4))[0] + mfma(a[3], B[3], mfma(a[1], B[1], z4))[0], ym);
    XST2(u32x2{asu(y), tag}, rs, yoff, 0, AUX_ST);
  }
  STAMP(6);
  STAMP_OUT();
}

extern "C" void kernel_launch(void* const* d_in, const int* in_sizes, int n_in, void* d_out, int out_size, void* d_ws, size_t ws_size,
                              hipStream_t stream) {
  Params p;
  p.x = (const char*)d_in[0]; p.k = (const char*)d_in[1]; p.v = (const char*)d_in[2]; p.wq = (const char*)d_in[3]; p.wo = (const char*)d_in[4];
  p.wg = (const char*)d_in[6]; p.wu = (const char*)d_in[7]; p.wd = (const char*)d_in[8]; p.gw = (const char*)d_in[9];
  p.out = (char*)d_out; p.ws = (char*)d_ws;
  hipLaunchKernelGGL(fused_decode, dim3(256), dim3(NTHR), 0, stream, p);
}
```

```cpp
#include <hip/hip_runtime.h>
#include <hip/hip_bf16.h>
#include <stdint.h>

typedef unsigned u32;
using u32x4 = __attribute__((ext_vector_type(4))) unsigned;
using u32x2 = __attribute__((ext_vector_type(2))) unsigned;
using f32x4 = __attribute__((ext_vector_type(4))) float;
using f32x2 = __attribute__((ext_vector_type(2))) float;
using bf16x2 = __attribute__((ext_vector_type(2))) __bf16;
using bf16x8 = __attribute__((ext_vector_type(8))) __bf16;
using s16x4 = __attribute__((ext_vector_type(4))) short;

constexpr int DM = 640, NH = 32, SK = 4096, WIN = 1024, DF = 1728;
constexpr int NTHR = 576;
#define AUX_LD 16
#define AUX_ST 16
#define KPRE 4
#define KMID 0
#define VPRE 5
#define NTH 4
#define NTV NHI
#define WLATE 6
#define QMID 3
#define RDELAY 16
#define MAPV 0
#define NHI 6
#define TB 250
#define WPOST 0
#define OFLAG 17
#define ODELAY 10
#define TP 110
#define PP 30
#define STG 4
#define PO 31
#define PN 31
#define QDELAY 16
constexpr u32 SPIN_CAP = 1u << 18;

constexpr int WS_E = 0;
constexpr int WS_QP = 4096;
constexpr int WS_GP = WS_QP + 32 * 4 * 2 * 128 * 8;
constexpr int OXS = 1152;
constexpr int WS_OX = WS_GP + 2 * 2 * 4 * DF * 8;
constexpr int WS_YP = WS_OX + 256 * OXS;
constexpr int WS_END = WS_YP + 4 * 5 * 32 * 128 * 8;
constexpr int OOB = 0x7ffffff0;

constexpr int L_XS = 0;
constexpr int L_R1 = L_XS + 8 * 160;
constexpr int L_QS = L_R1 + 2048;
constexpr int L_HS = L_QS + 64;
constexpr int L_OS = L_HS + 128;
constexpr int L_MISC = L_OS + 256;
constexpr int L_FLG = L_MISC + 8;
constexpr int L_ML = L_FLG + 36;
constexpr int L_FW = L_ML + 16;
constexpr int L_OWN = L_FW + 8;
constexpr int L_REC = L_OWN + 128;
constexpr int WROW = 72;
constexpr int L_W = L_REC + 1024;
constexpr int L_END = L_W + 192 * WROW;

struct Params {
  const char* x; const char* k; const char* v; const char* wq; const char* wo;
  const char* wg; const char* wu; const char* wd; const char* gw; char* out; char* ws;
};

#define STAMP(i)
#define STAMPV(i, v)
#define STAMP_DECL
#define STAMP_OUT()
#define SB() __builtin_amdgcn_sched_barrier(0)
#define PIN8(a) asm volatile("" : "+v"(a[0]), "+v"(a[1]), "+v"(a[2]), "+v"(a[3]), "+v"(a[4]), "+v"(a[5]), "+v"(a[6]), "+v"(a[7]))
#define THR(n) do { SB(); __builtin_amdgcn_s_waitcnt(((n) & 15) | (7 << 4) | (15 << 8) | (((n) >> 4) << 14)); SB(); } while (0)
#define UNTIL(t_) do { const u32 t1_ = (t_) - 1u; while (t1_ - (u32)__builtin_amdgcn_s_memrealtime() < 2000u) {} } while (0)
#define THRA() do { THR(NHI - 1); UNTIL(due); due += pace; SB(); } while (0)
#define CLOB() asm volatile("" ::: "memory")
#define WG_BARRIER() do { asm volatile("s_waitcnt lgkmcnt(0)" ::: "memory"); __builtin_amdgcn_s_barrier(); CLOB(); } while (0)
#define XST __builtin_amdgcn_raw_buffer_store_b128
#define XST2 __builtin_amdgcn_raw_buffer_store_b64
#define XLD4 __builtin_amdgcn_raw_buffer_load_b128

__device__ __forceinline__ u32x4 ldnt(const char* p) { return __builtin_nontemporal_load(reinterpret_cast<const u32x4*>(p)); }
__device__ __forceinline__ float asf(u32 x) { return __builtin_bit_cast(float, x); }
__device__ __forceinline__ u32 asu(float x) { return __builtin_bit_cast(u32, x); }
__device__ __forceinline__ float lo16(u32 x) { return asf(x << 16); }
__device__ __forceinline__ float hi16(u32 x) { return asf(x & 0xffff0000u); }
__device__ __forceinline__ float dot2(u32 a, u32 b, float acc) {
  return __builtin_amdgcn_fdot2_f32_bf16(__builtin_bit_cast(bf16x2, a), __builtin_bit_cast(bf16x2, b), acc, false);
}
__device__ __forceinline__ u32 pkbf(float lo, float hi) {
  bf16x2 r; r[0] = (__bf16)lo; r[1] = (__bf16)hi; return __builtin_bit_cast(u32, r);
}
__device__ __forceinline__ float rbf(float x) { return lo16(pkbf(x, 0.f)); }
template <int CTRL> __device__ __forceinline__ float dpp(float x) {
  return asf(__builtin_amdgcn_mov_dpp(__builtin_bit_cast(int, x), CTRL, 0xf, 0xf, true));
}
constexpr int XOR1 = 0xB1, XOR2 = 0x4E, XOR7 = 0x141, XOR8 = 0x128, BCAST = 0x150;
__device__ __forceinline__ float red32(float a, float b) {
  auto s = __builtin_amdgcn_permlane32_swap(asu(a), asu(b), false, false); return asf(s[0]) + asf(s[1]);
}
__device__ __forceinline__ float red16(float a, float b) {
  auto s = __builtin_amdgcn_permlane16_swap(asu(a), asu(b), false, false); return asf(s[0]) + asf(s[1]);
}
__device__ __forceinline__ float max32(float a) {
  auto s = __builtin_amdgcn_permlane32_swap(asu(a), asu(a), false, false); return fmaxf(asf(s[0]), asf(s[1]));
}
__device__ __forceinline__ float max16(float a) {
  auto s = __builtin_amdgcn_permlane16_swap(asu(a), asu(a), false, false); return fmaxf(asf(s[0]), asf(s[1]));
}
__device__ __forceinline__ float wave_sum(float s) {
  s += dpp<XOR1>(s); s += dpp<XOR2>(s); s += dpp<XOR7>(s); s += dpp<XOR8>(s); s = red16(s, s); return red32(s, s);
}
__device__ __forceinline__ float geglu(float g, float u) {
  const float ge = rbf(0.5f * g * (1.f + tanhf(0.7978845608028654f * (g + 0.044715f * g * g * g))));
  return ge * u;
}

__device__ __forceinline__ void pv_step(float (&acc)[8], float p, const u32x4& v) {
#pragma unroll
  for (int k = 0; k < 4; ++k) { acc[2 * k] = fmaf(p, lo16(v[k]), acc[2 * k]); acc[2 * k + 1] = fmaf(p, hi16(v[k]), acc[2 * k + 1]); }
}
__device__ __forceinline__ f32x4 gemv_mfma(f32x4 d, u32 mv, const u32x4& mk, const u32x4& w) {
  const u32x4 a = {mv & mk[0], mv & mk[1], mv & mk[2], mv & mk[3]};
  return __builtin_amdgcn_mfma_f32_16x16x32_bf16(__builtin_bit_cast(bf16x8, a), __builtin_bit_cast(bf16x8, w), d, 0, 0, 0);
}
__device__ __forceinline__ f32x4 mfma(const u32x4& a, const u32x4& b, f32x4 d) {
  return __builtin_amdgcn_mfma_f32_16x16x32_bf16(__builtin_bit_cast(bf16x8, a), __builtin_bit_cast(bf16x8, b), d, 0, 0, 0);
}
__device__ __forceinline__ u32x2 lds_tr(const u32* p) {
  return __builtin_bit_cast(u32x2, __builtin_amdgcn_ds_read_tr16_b64_v4i16((__attribute__((address_space(3))) s16x4*)p));
}
template <int N, int NAP>
__device__ __forceinline__ u32 gather(const __amdgpu_buffer_rsrc_t rs, int (&off)[N], u32x4 (&g)[N], u32 tag) {
  u32 spins = 0;
#pragma unroll 1
  for (;; ++spins) {
    CLOB();
    u32x4 n[N];
#pragma unroll
    for (int u = 0; u < N; ++u) n[u] = XLD4(rs, off[u], 0, AUX_LD);
    bool ok = true;
#pragma unroll
    for (int u = 0; u < N; ++u) {
      const bool live = off[u] != OOB, hit = (n[u][1] == tag) & (n[u][3] == tag);
      g[u][0] = live ? n[u][0] : g[u][0]; g[u][2] = live ? n[u][2] : g[u][2];
      off[u] = hit ? OOB : off[u];
      ok &= hit | !live;
    }
    if (__all(ok) || spins > SPIN_CAP) break;
    __builtin_amdgcn_s_sleep(NAP);
  }
  return spins;
}
template <int I> struct PV {
  static __device__ __forceinline__ float bc(float p) { return dpp<BCAST + I>(p); }
};

typedef volatile __attribute__((address_space(3))) u32 vlds_u32;
__device__ __forceinline__ void lds_set(u32* f, int lane) { CLOB(); if (lane == 0) *(vlds_u32*)f = 1u; CLOB(); }
template <int NAP>
__device__ __forceinline__ void lds_wait(u32* f, int n, int lane) {
  vlds_u32* q = (vlds_u32*)(f + (lane < n ? lane : 0));
#pragma unroll 1
  for (u32 spins = 0; spins < (SPIN_CAP << 4); ++spins) { if (__all(*q != 0u)) break; if (NAP) __builtin_amdgcn_s_sleep(NAP); }
  CLOB();
}

__global__ void __launch_bounds__(NTHR, 3) fused_decode(Params p) {
  __shared__ __attribute__((aligned(16))) float lds[L_END];
  const int bid = blockIdx.x, tid = threadIdx.x, lane = tid & 63;
  const int wave = __builtin_amdgcn_readfirstlane(tid >> 6);
  const int xcd = bid & 7, slot = bid >> 3;
  const int h = xcd * 4 + (slot >> 3), t = slot & 7;
  const int b = t >> 1, j = t & 1, wg = h * 8 + t;
  const bool isq = t >= 4;
  const int mt = t * 32 + h;
  const bool s1on = isq || mt < 108;
  const int mm = mt >= 54 ? 1 : 0, mc = (mt - 54 * mm) >> 1, rh = isq ? ((t >> 1) & 1) : (mt & 1);
  const bool wown = t < 5;
  const __amdgpu_buffer_rsrc_t rs = __builtin_amdgcn_make_buffer_rsrc((void*)p.ws, 0, WS_END, 0x00020000);
  u32* ldsu = reinterpret_cast<u32*>(lds);
  u32* flg = ldsu + L_FLG;
  STAMP_DECL;
  STAMP(0);
  u32 due = (u32)__builtin_amdgcn_s_memrealtime() + TB + (wave - 4) * STG;
  const u32 pace = wown ? PO : PN;

  const int row0 = 320 * rh + 40 * (wave & 7);
  u32x4 S1[5], K[16], V[16], W[6];
  u32x2 xr = {0u, 0u};
  u32 ev = 0u;
  const int xb = min(lane / 10, 3), xc = min(lane - 10 * xb, 9);
  const size_t kvoff = ((size_t)(b * NH + h) * SK + (SK - WIN) + j * 512 + (wave & 7) * 64) * 256 + lane * 16;
  const char* kp = p.k + kvoff;
  const char* vp = p.v + kvoff;
  if (wave < 8) {
    xr = *reinterpret_cast<const u32x2*>(p.x + xb * (DM * 2) + (row0 + 4 * xc) * 2);
    const char* sp; int sstep;
    if (isq) { sp = p.wq + (size_t)(row0 + (lane >> 3)) * 8192 + h * 256 + (t & 1) * 128 + (lane & 7) * 16; sstep = 8 * 8192; }
    else { sp = (mm ? p.wu : p.wg) + (size_t)(row0 + (lane >> 3)) * (DF * 2) + mc * 128 + (lane & 7) * 16; sstep = 8 * DF * 2; }
    if (!s1on) { sp = p.x + (lane & 15) * 16; sstep = 0; }
#pragma unroll
    for (int i = 0; i < 5; ++i) S1[i] = ldnt(sp + i * sstep);
    SB();
    STAMP(18);
  } else {
    ev = __builtin_amdgcn_raw_buffer_load_b32(rs, WS_E, 0, 0);
    if (lane < 36) flg[lane] = 0u;
  }
  SB();
  WG_BARRIER();
  SB();
  STAMP(20);

  if (__builtin_expect(wave == 8, 0)) {
    __builtin_amdgcn_s_setprio(3);
    const u32 tag = __builtin_amdgcn_readfirstlane(ev) + 1u;
    lds_wait<1>(flg + 0, 8, lane);
    STAMP(11);
    if (s1on) {
#pragma unroll
      for (int u = 0; u < 2; ++u) {
        const float* d = lds + L_R1 + u * 128 + lane * 2;
        f32x2 s = *reinterpret_cast<const f32x2*>(d);
#pragma unroll
        for (int w = 1; w < 8; ++w) s += *reinterpret_cast<const f32x2*>(d + w * 256);
        const int bb = 2 * u + (lane >> 5);
        const int off = isq ? WS_QP + ((h * 4 + bb) * 2 + rh) * 1024 + (t & 1) * 512 : WS_GP + (((mm * 2 + rh) * 4 + bb) * DF + mc * 64) * 8;
        XST(u32x4{asu(s[0]), tag, asu(s[1]), tag}, rs, off + (lane & 31) * 16, 0, AUX_ST);
      }
    }
    STAMP(1);
    __builtin_amdgcn_s_sleep(QDELAY);
    {
      u32x4 g[2];
      int off[2];
#pragma unroll
      for (int u = 0; u < 2; ++u) { off[u] = WS_QP + ((h * 4 + b) * 2 + u) * 1024 + lane * 16; g[u] = u32x4{0u, 0u, 0u, 0u}; }
      const u32 sp_ = gather<2, 2>(rs, off, g, tag);
      STAMPV(12, sp_); (void)sp_;
      ldsu[L_QS + lane] = pkbf(asf(g[0][0]) + asf(g[1][0]), asf(g[0][2]) + asf(g[1][2]));
      if (lane == 0) ldsu[L_MISC + 4] = tag;
      lds_set(flg + 24, lane);
      STAMP(7);
    }
    if (wown) {
      const int i = lane & 31, m = lane >> 5;
      {
        float s = 0.f;
#pragma unroll
        for (int c = 0; c < 5; ++c) {
          const u32x4 xv = *reinterpret_cast<const u32x4*>(p.x + (lane >> 4) * (DM * 2) + (c * 16 + (lane & 15)) * 16);
          const u32x4 gv = *reinterpret_cast<const u32x4*>(p.gw + (c * 16 + (lane & 15)) * 16);
#pragma unroll
          for (int k = 0; k < 4; ++k) s = dot2(xv[k], gv[k], s);
        }
        s += dpp<XOR1>(s); s += dpp<XOR2>(s); s += dpp<XOR7>(s); s += dpp<XOR8>(s);
        if ((lane & 15) == 0) lds[L_MISC + (lane >> 4)] = 1.f / (1.f + __expf(-s));
      }
      u32x4 g[8];
      int off[8];
#pragma unroll
      for (int u = 0; u < 8; ++u) { off[u] = i < 27 ? WS_GP + ((m * 8 + u) * DF + 54 * h + 2 * i) * 8 : OOB; g[u] = u32x4{0u, 0u, 0u, 0u}; }
      gather<8, 8>(rs, off, g, tag);
#pragma unroll
      for (int bb = 0; bb < 4; ++bb) {
        const float a0 = rbf(asf(g[bb][0]) + asf(g[4 + bb][0])), a1 = rbf(asf(g[bb][2]) + asf(g[4 + bb][2]));
        auto s = __builtin_amdgcn_permlane32_swap(asu(a0), asu(a1), false, false);
        const float hv = geglu(asf(s[0]), asf(s[1]));
        reinterpret_cast<unsigned short*>(ldsu + L_HS)[bb * 64 + 2 * i + m] = (unsigned short)pkbf(hv, 0.f);
      }
      lds_set(flg + 25, lane);
    }
    STAMP(8);
    lds_wait<1>(flg + 8, 8, lane);
    {
      const f32x2 ml = *reinterpret_cast<const f32x2*>(lds + L_ML + 2 * (lane & 7));
      float M = ml[0];
      M = fmaxf(M, dpp<XOR1>(M)); M = fmaxf(M, dpp<XOR2>(M)); M = fmaxf(M, dpp<XOR7>(M));
      const float fw = __builtin_amdgcn_exp2f(ml[0] - M);
      float L = fw * ml[1];
      L += dpp<XOR1>(L); L += dpp<XOR2>(L); L += dpp<XOR7>(L);
      if (lane < 8) lds[L_FW + lane] = fw;
      lds_set(flg + 18, lane);
      if (lane == 0) XST(u32x4{asu(M), tag, asu(L), tag}, rs, WS_OX + wg * OXS + 1024, 0, AUX_ST);
    }
    STAMP(2);
    if (wown) {
      float c[8];
      {
        u32x4 g[1] = {u32x4{0u, 0u, 0u, 0u}};
        int off[1] = {lane < 8 ? WS_OX + (h * 8 + lane) * OXS + 1024 : OOB};
        __builtin_amdgcn_s_sleep(16);
        gather<1, 8>(rs, off, g, tag);
        const float mu = asf(g[0][0]);
        float sc = __builtin_amdgcn_exp2f(mu - fmaxf(mu, dpp<XOR1>(mu)));
        const float sl = sc * asf(g[0][2]);
        sc = sc / (sl + dpp<XOR1>(sl));
#pragma unroll
        for (int u = 0; u < 8; ++u) c[u] = asf(__builtin_amdgcn_readlane(asu(sc), u));
      }
      STAMP(19);
      lds_wait<1>(flg + OFLAG, 1, lane);
      if (ODELAY) __builtin_amdgcn_s_sleep(ODELAY);
      u32x4 g[8];
#pragma unroll
      for (int u = 0; u < 8; ++u) g[u] = u32x4{0u, 0u, 0u, 0u};
      const int off = WS_OX + h * 8 * OXS + lane * 16;
      u32 miss = 0xffu, todo = 0xfu, spins = 0;
#pragma unroll 1
      for (;; ++spins) {
        CLOB();
        const u32 far = miss & ~(1u << t);
#pragma unroll
        for (int u = 0; u < 8; ++u) if ((far >> u) & 1) g[u] = XLD4(rs, off + u * OXS, 0, AUX_LD);
#pragma unroll
        for (int u = 0; u < 8; ++u) if ((far >> u) & 1) { if (__all((g[u][1] == tag) & (g[u][3] == tag))) miss &= ~(1u << u); }
        if ((miss >> t) & 1) {
          const u32 fl = *(vlds_u32*)(flg + 17);
          CLOB();
          const u32x2 own = *reinterpret_cast<const u32x2*>(ldsu + L_OWN + 2 * lane);
          if (__builtin_amdgcn_readfirstlane(fl)) {
            miss &= ~(1u << t);
#pragma unroll
            for (int u = 0; u < 5; ++u) if (u == t) { g[u][0] = own[0]; g[u][2] = own[1]; }
          }
        }
        if (spins > SPIN_CAP) miss = 0u;
#pragma unroll
        for (int bb = 0; bb < 4; ++bb) if (((todo >> bb) & 1) && !((miss >> (2 * bb)) & 3)) {
          const float o0 = fmaf(c[2 * bb], asf(g[2 * bb][0]), c[2 * bb + 1] * asf(g[2 * bb + 1][0]));
          const float o1 = fmaf(c[2 * bb], asf(g[2 * bb][2]), c[2 * bb + 1] * asf(g[2 * bb + 1][2]));
          ldsu[L_OS + bb * 64 + lane] = pkbf(o0, o1);
          todo &= ~(1u << bb);
        }
        if (!miss) break;
      }
      lds_set(flg + 26, lane);
      STAMP(10);
      STAMPV(17, spins);
    } else {
      const int r = h * 3 + t - 5;
      if (r < 80) {
        const int rb = r / 20, rc = r - 20 * rb;
        u32x4 g[8];
#pragma unroll
        for (int u = 0; u < 8; ++u) g[u] = u32x4{0u, 0u, 0u, 0u};
        const int off = WS_YP + ((rb * 5 + (rc >> 2)) * 32 + (lane >> 4)) * 1024 + (rc & 3) * 256 + (lane & 15) * 16;
        lds_wait<1>(flg + 17, 1, lane);
        __builtin_amdgcn_s_sleep(RDELAY);
        STAMP(15);
        u32 miss = 0xffu, spins = 0;
#pragma unroll 1
        for (;; ++spins) {
          CLOB();
          const u32 far = miss;
#pragma unroll
          for (int u = 0; u < 8; ++u) if ((far >> u) & 1) g[u] = XLD4(rs, off + u * 4096, 0, AUX_LD);
#pragma unroll
          for (int u = 0; u < 8; ++u) if ((far >> u) & 1) { if (__all((g[u][1] == tag) & (g[u][3] == tag))) miss &= ~(1u << u); }
          if (!miss || spins > SPIN_CAP) break;
        }
        STAMPV(16, spins);
        float y0 = ((asf(g[0][0]) + asf(g[1][0])) + (asf(g[2][0]) + asf(g[3][0]))) + ((asf(g[4][0]) + asf(g[5][0])) + (asf(g[6][0]) + asf(g[7][0])));
        float y1 = ((asf(g[0][2]) + asf(g[1][2])) + (asf(g[2][2]) + asf(g[3][2]))) + ((asf(g[4][2]) + asf(g[5][2])) + (asf(g[6][2]) + asf(g[7][2])));
        y0 = red16(y0, y0); y1 = red16(y1, y1);
        y0 = red32(y0, y0); y1 = red32(y1, y1);
        if (lane < 16) *reinterpret_cast<u32*>(p.out + (rb * DM + 32 * rc + 2 * lane) * 2) = pkbf(y0, y1);
        STAMP(9);
      } else if (wg == 255) {
        u32x4 g[4];
        int off[4];
#pragma unroll
        for (int u = 0; u < 4; ++u) { off[u] = WS_OX + (u * 64 + lane) * OXS + 1024; g[u] = u32x4{0u, 0u, 0u, 0u}; }
        gather<4, 4>(rs, off, g, tag);
        if (lane == 0) __builtin_amdgcn_raw_buffer_store_b32(tag, rs, WS_E, 0, AUX_ST);
      }
    }
    STAMP_OUT();
    return;
  }

  u32x4 mk;
#pragma unroll
  for (int k = 0; k < 4; ++k) mk[k] = ((lane >> 1) & 3) == k ? ((lane & 1) ? 0xffff0000u : 0xffffu) : 0u;
  {
    u32* xs = ldsu + L_XS + wave * 160;
#pragma unroll
    for (int k = 0; k < 2; ++k) {
      xs[(4 * xc + 2 * k) * 4 + xb] = __builtin_amdgcn_perm(xr[k], xr[k], 0x01000100u);
      xs[(4 * xc + 2 * k + 1) * 4 + xb] = __builtin_amdgcn_perm(xr[k], xr[k], 0x03020302u);
    }
    asm volatile("s_waitcnt lgkmcnt(0)" ::: "memory");
    u32x4 xv[5];
#pragma unroll
    for (int i = 0; i < 5; ++i) xv[i] = *reinterpret_cast<const u32x4*>(xs + ((lane >> 3) + i * 8) * 4);
    f32x4 d[4];
#pragma unroll
    for (int bb = 0; bb < 4; ++bb) d[bb] = f32x4{0.f, 0.f, 0.f, 0.f};
    SB();
#pragma unroll
    for (int i = 0; i < KPRE; ++i) {
      UNTIL(due - TB + TP + i * PP);
      K[i] = ldnt(kp + i * 1024); SB();
    }
#pragma unroll
    for (int i = 0; i < 5; ++i) {
      if (i == 0) { THR(4 + KPRE); STAMP(21); }
      if (i < KMID) { THR(4 + KPRE); K[KPRE + i] = ldnt(kp + (KPRE + i) * 1024); SB(); }
#pragma unroll
      for (int bb = 0; bb < 4; ++bb) d[bb] = gemv_mfma(d[bb], xv[i][bb], mk, S1[i]);
    }
    STAMP(22);
    f32x4 r;
#pragma unroll
    for (int k = 0; k < 4; ++k) {
      auto s0 = __builtin_amdgcn_permlane32_swap(asu(d[0][k]), asu(d[1][k]), false, false);
      auto s1 = __builtin_amdgcn_permlane32_swap(asu(d[2][k]), asu(d[3][k]), false, false);
      const float t0 = asf(s0[0]) + dpp<XOR8>(asf(s0[1])), t1 = asf(s1[1]) + dpp<XOR8>(asf(s1[0]));
      r[k] = (lane & 8) ? t1 : t0;
    }
    *reinterpret_cast<f32x4*>(lds + L_R1 + wave * 256 + (((lane >> 3) & 1) * 2 + (lane >> 5)) * 64 + (lane & 7) * 8 + ((lane >> 4) & 1) * 4) = r;
    lds_set(flg + wave, lane);
  }
  SB();
  const __amdgpu_buffer_rsrc_t rd = __builtin_amdgcn_make_buffer_rsrc((void*)(p.wd + (size_t)(54 * h) * (DM * 2)), 0, wown ? 54 * DM * 2 : 0, 0x00020000);
  const __amdgpu_buffer_rsrc_t ro = __builtin_amdgcn_make_buffer_rsrc((void*)(p.wo + (size_t)(128 * h) * (DM * 2)), 0, wown ? 128 * DM * 2 : 0, 0x00020000);
  const int wdo = (8 * wave + (lane >> 4)) * (DM * 2) + 256 * t + (lane & 15) * 16;
  const int woo = (16 * wave + (lane >> 4)) * (DM * 2) + 256 * t + (lane & 15) * 16;
#define CL(n_, lo_, hi_) ((n_) >= (lo_) && (n_) < (hi_) ? (n_) - (lo_) : 0)
#define SLOAD(n_) do { \
    if ((n_) < 16) V[CL(n_, 0, 16)] = ldnt(vp + (n_) * 1024); \
    else if ((n_) < 18) W[CL(n_, 16, 22)] = __builtin_amdgcn_raw_buffer_load_b128(rd, wdo + ((n_) - 16) * (DM * 8), 0, 2); \
    else if ((n_) < 22) W[CL(n_, 16, 22)] = __builtin_amdgcn_raw_buffer_load_b128(ro, woo + ((n_) - 18) * (DM * 8), 0, 2); \
  } while (0)
#pragma unroll
  for (int i = KPRE + KMID; i < 16; ++i) { THRA(); K[i] = ldnt(kp + i * 1024); }
  STAMP(13);
#pragma unroll
  for (int i = 0; i < VPRE; ++i) { THRA(); SLOAD(i); }
  SB();
  lds_wait<1>(flg + 24, 1, lane);
  float pe;
  {
    const u32x4 q = *reinterpret_cast<const u32x4*>(ldsu + L_QS + (lane & 15) * 4);
    float x[16];
#pragma unroll
    for (int i = 0; i < 16; ++i) { float d = 0.f;
#pragma unroll
      for (int k = 0; k < 4; ++k) d = dot2(K[i][k], q[k], d);
      x[i] = d;
      if ((i + 1) * QMID / 16 > i * QMID / 16) { asm volatile("" : "+v"(x[i])); THRA(); SLOAD(VPRE + i * QMID / 16); SB(); }
    }
#define TR_STEP(HALF, CTRL) _Pragma("unroll") for (int u = 0; u < (HALF); ++u) { \
      const float t1_ = x[u] + dpp<CTRL>(x[u]), t2_ = x[u + (HALF)] + dpp<CTRL>(x[u + (HALF)]); x[u] = (lane & (HALF)) ? t2_ : t1_; }
    TR_STEP(8, XOR8) TR_STEP(4, XOR7) TR_STEP(2, XOR2) TR_STEP(1, XOR1)
#undef TR_STEP
    const float s = x[0] * (0.08838834764831845f * 1.4426950408889634f);
    float m = s;
    m = fmaxf(m, dpp<XOR1>(m)); m = fmaxf(m, dpp<XOR2>(m)); m = fmaxf(m, dpp<XOR7>(m)); m = fmaxf(m, dpp<XOR8>(m)); m = max16(m); m = max32(m);
    pe = __builtin_amdgcn_exp2f(s - m);
    const float l = wave_sum(pe);
    if (lane == 0) *reinterpret_cast<f32x2*>(lds + L_ML + 2 * wave) = f32x2{m, l};
    lds_set(flg + 8 + wave, lane);
  }
  STAMP(4);
  SB();
  float acc[8];
#pragma unroll
  for (int k = 0; k < 8; ++k) acc[k] = 0.f;
  constexpr int WE = 6 - WLATE, VL = VPRE + QMID - 1;
#define SEQ(n_) ((n_) <= VL ? (n_) : (n_) <= VL + WE ? (n_) + 15 - VL : (n_) <= 15 + WE ? (n_) - WE : (n_))
#define MIN22(a_) ((a_) < 22 - WPOST ? (a_) : 22 - WPOST)
#define ISS(i_) ((i_) > VL ? MIN22((i_) + WE + NTV) : VL + 1 + ((i_) * (WE + NTV) + VL) / (VL + 1))
#define PVS(i_) do { _Pragma("unroll") for (int n_ = ISS(i_); n_ < ISS((i_) + 1); ++n_) { if (SEQ(n_) < 16) THRA(); else THR(NHI - 1); SLOAD(SEQ(n_)); } \
    SB(); pv_step(acc, PV<i_>::bc(pe), V[i_]); PIN8(acc); SB(); } while (0)
  PVS(0); PVS(1); PVS(2); PVS(3); PVS(4); PVS(5); PVS(6); PVS(7);
  PVS(8); PVS(9); PVS(10); PVS(11); PVS(12); PVS(13);
  lds_wait<1>(flg + 18, 1, lane);
  const float fw = lds[L_FW + wave];
  const u32 tag = ldsu[L_MISC + 4];
  PVS(14); PVS(15);
#pragma unroll
  for (int i = ISS(16); i < 22 - WPOST; ++i) SLOAD(SEQ(i));
  SB();
  {
    float u4[4];
#pragma unroll
    for (int k = 0; k < 4; ++k) u4[k] = red32(acc[k], acc[k + 4]);
    *reinterpret_cast<f32x2*>(lds + L_REC + wave * 128 + 8 * (lane & 15) + 2 * (lane >> 4)) = f32x2{fw * red16(u4[0], u4[2]), fw * red16(u4[1], u4[3])};
    CLOB();
    u32 cnt = 0u;
    if (lane == 0) cnt = __hip_atomic_fetch_add(flg + 16, 1u, __ATOMIC_RELAXED, __HIP_MEMORY_SCOPE_WORKGROUP);
    CLOB();
    f32x2 r[8];
#pragma unroll
    for (int w = 0; w < 8; ++w) r[w] = *reinterpret_cast<const f32x2*>(lds + L_REC + w * 128 + lane * 2);
    asm volatile("" : "+v"(r[0]), "+v"(r[1]), "+v"(r[2]), "+v"(r[3]), "+v"(r[4]), "+v"(r[5]), "+v"(r[6]), "+v"(r[7]));
    if (__builtin_amdgcn_readfirstlane(cnt) == 7u) {
      const f32x2 o = ((r[0] + r[1]) + (r[2] + r[3])) + ((r[4] + r[5]) + (r[6] + r[7]));
      XST(u32x4{asu(o[0]), tag, asu(o[1]), tag}, rs, WS_OX + wg * OXS + lane * 16, 0, AUX_ST);
      *reinterpret_cast<f32x2*>(lds + L_OWN + 2 * lane) = o;
      lds_set(flg + 17, lane);
      STAMP(14);
    }
  }
  STAMP(5);
  SB();
  if (!wown) { STAMP_OUT(); return; }
#pragma unroll
  for (int i = 22 - WPOST; i < 22; ++i) SLOAD(SEQ(i));
  SB();
  {
    u32* wr = ldsu + L_W + (lane >> 4) * WROW + (lane & 15) * 4;
#pragma unroll
    for (int n = 0; n < 2; ++n) *reinterpret_cast<u32x4*>(wr + (128 + 8 * wave + 4 * n) * WROW) = W[n];
#pragma unroll
    for (int n = 0; n < 4; ++n) *reinterpret_cast<u32x4*>(wr + (16 * wave + 4 * n) * WROW) = W[2 + n];
    lds_set(flg + 28 + wave, lane);
  }
  lds_wait<1>(flg + 28, 8, lane);
  u32x4 B[6];
  {
    const u32* rd_ = ldsu + L_W + (8 * (lane >> 4) + ((lane >> 2) & 3)) * WROW + 8 * wave + 2 * (lane & 3);
#pragma unroll
    for (int s = 0; s < 6; ++s) { const u32x2 lo = lds_tr(rd_ + 32 * s * WROW), hi = lds_tr(rd_ + (32 * s + 4) * WROW); B[s] = u32x4{lo[0], lo[1], hi[0], hi[1]}; }
  }
  lds_wait<1>(flg + 25, 1, lane);
  const int g = lane >> 4;
  const float gate = lds[L_MISC + g];
  const f32x4 z4 = {0.f, 0.f, 0.f, 0.f};
  float ym;
  {
    const u32* hs = ldsu + L_HS + ((lane >> 2) & 3) * 32 + 4 * g;
    ym = (1.f - gate) * mfma(*reinterpret_cast<const u32x4*>(hs + 16), B[5], mfma(*reinterpret_cast<const u32x4*>(hs), B[4], z4))[0];
  }
  int yoff = WS_YP + ((g * 5 + t) * 32 + h) * 1024 + wave * 128 + (lane & 15) * 8;
  asm volatile("" : "+v"(ym), "+v"(yoff), "+v"(B[0]), "+v"(B[1]), "+v"(B[2]), "+v"(B[3]));
  STAMP(3);
  SB();
  {
    const u32* os = ldsu + L_OS + ((lane >> 2) & 3) * 64 + 4 * g;
    u32x4 a[4];
#pragma unroll 1
    for (u32 spins = 0; spins < (SPIN_CAP << 4); ++spins) {
      const u32 fl = *(vlds_u32*)(flg + 26);
      CLOB();
#pragma unroll
      for (int s = 0; s < 4; ++s) a[s] = *reinterpret_cast<const u32x4*>(os + 16 * s);
      asm volatile("" : "+v"(a[0]), "+v"(a[1]), "+v"(a[2]), "+v"(a[3]) :: "memory");
      if (__builtin_amdgcn_readfirstlane(fl)) break;
    }
    const float y = fmaf(gate, mfma(a[2], B[2], mfma(a[0], B[0], z4))[0] + mfma(a[3], B[3], mfma(a[1], B[1], z4))[0], ym);
    XST2(u32x2{asu(y), tag}, rs, yoff, 0, AUX_ST);
  }
  STAMP(6);
  STAMP_OUT();
}

extern "C" void kernel_launch(void* const* d_in, const int* in_sizes, int n_in, void* d_out, int out_size, void* d_ws, size_t ws_size,
                              hipStream_t stream) {
  Params p;
  p.x = (const char*)d_in[0]; p.k = (const char*)d_in[1]; p.v = (const char*)d_in[2]; p.wq = (const char*)d_in[3]; p.wo = (const char*)d_in[4];
  p.wg = (const char*)d_in[6]; p.wu = (const char*)d_in[7]; p.wd = (const char*)d_in[8]; p.gw = (const char*)d_in[9];
  p.out = (char*)d_out; p.ws = (char*)d_ws;
  hipLaunchKernelGGL(fused_decode, dim3(256), dim3(NTHR), 0, stream, p);
}
```
